```python
import torch

def _softmax(x, axis=-1):
    return torch.softmax(x, dim=axis)

def _gelu(x):
    return torch.nn.functional.gelu(x, approximate='tanh')

def _silu(x):
    return torch.nn.functional.silu(x)

def _sigmoid(x):
    return torch.sigmoid(x)

def _layernorm(x, axis=-1, eps=1e-05):
    m = x.mean(dim=axis, keepdim=True)
    v = x.var(dim=axis, keepdim=True, unbiased=False)
    return (x - m) / torch.sqrt(v + eps)

def _scatter_add(tgt, idx, src, axis=0):
    return tgt.clone().index_add_(axis, idx, src)

def _segment_sum(src, seg, n):
    out = torch.zeros((n,) + src.shape[1:], dtype=src.dtype)
    return out.index_add_(0, seg, src)

def _pad(x, pw):
    flat = tuple((v for pair in reversed(pw) for v in pair))
    return torch.nn.functional.pad(x, flat)
kernel_name = 'adaLN_cond__ssm_conv_h3__mixer_s_mlp__prompt'

def setup_inputs():

    _w = lambda a: (lambda t: t.to(torch.bfloat16) if t.is_floating_point() else t)(torch.as_tensor(a))

    def _A_setup():
        B, S, D = (1, 2048, 512)
        ks = 4
        return {'q': _w((lambda *s: torch.randn(*s, dtype=torch.bfloat16) * 0.05)(B, S, D)), 'k': _w((lambda *s: torch.randn(*s, dtype=torch.bfloat16) * 0.8)(B, S, D)), 'v': _w((lambda *s: torch.randn(*s, dtype=torch.bfloat16) * 0.8)(B, S, D)), 'shift_k': _w((lambda *s: torch.randn(*s, dtype=torch.bfloat16) * 0.8)(D, ks) * 0.5), 'ssm_k': _w((lambda *s: torch.randn(*s, dtype=torch.bfloat16) * 0.8)(D, S) * (1.0 / S ** 0.5)), 'ssm_D': _w((lambda *s: torch.randn(*s, dtype=torch.bfloat16) * 0.8)(D))}
    _A_in = _A_setup()

    def _B_setup():
        B, S, D, F = (1, 2048, 512, 1792)
        return {'x': _w((lambda *s: torch.randn(*s, dtype=torch.bfloat16) * 0.05)(B, S, D)), 'w1': _w((lambda *s: torch.randn(*s, dtype=torch.bfloat16) * 3.2)(D, D) * D ** (-0.5)), 'w2': _w((lambda *s: torch.randn(*s, dtype=torch.bfloat16) * 3.2)(D, D) * D ** (-0.5)), 'w3': _w((lambda *s: torch.randn(*s, dtype=torch.bfloat16) * 3.2)(D, F) * D ** (-0.5)), 'w4': _w((lambda *s: torch.randn(*s, dtype=torch.bfloat16) * 3.2)(F, D) * F ** (-0.5))}
    _B_in = _B_setup()
    return {'A_q': _A_in['q'], 'A_k': _A_in['k'], 'A_v': _A_in['v'], 'A_shift_k': _A_in['shift_k'], 'A_ssm_k': _A_in['ssm_k'], 'A_ssm_D': _A_in['ssm_D'], 'B_x': _B_in['x'], 'B_w1': _B_in['w1'], 'B_w2': _B_in['w2'], 'B_w3': _B_in['w3'], 'B_w4': _B_in['w4'], 'c': _w((lambda *s: torch.randn(*s, dtype=torch.bfloat16) * 0.1)(1, 512) * 8.0), 'w_mod': _w((lambda *s: torch.randn(*s, dtype=torch.bfloat16) * 0.1)(512, 6 * 512) * 512 ** (-0.5) * 8.0)}

def reference(A_q, A_k, A_v, A_shift_k, A_ssm_k, A_ssm_D, B_x, B_w1, B_w2, B_w3, B_w4, c, w_mod):

    def _A_ref(q, k, v, shift_k, ssm_k, ssm_D):
        B, S, D = q.shape
        ks = shift_k.shape[-1]
        k_pad = _pad(k, ((0, 0), (ks - 1, 0), (0, 0)))
        k_shift = torch.zeros_like(k)
        for t in range(ks):
            k_shift = k_shift + k_pad[:, t:t + S, :] * shift_k[:, ks - 1 - t][None, None, :]
        kv = k_shift * v
        L = 2 * S
        kv_pad = _pad(kv, ((0, 0), (0, S), (0, 0)))
        kf_pad = _pad(ssm_k, ((0, 0), (0, S)))
        kv_f = (lambda x, axes=(-2, -1): torch.fft.rfft2(x.float(), dim=axes))(kv_pad[:, None, :, :], axes=(1, 2))
        kf_f = (lambda x, axes=(-2, -1): torch.fft.rfft2(x.float(), dim=axes))(kf_pad[:, None, :], axes=(1, 2))
        kf_f = (lambda x, *d: x.permute(*d))(kf_f, 1, 2, 0)
        y_f = kv_f * kf_f[None, :, :, :]
        y = (lambda x, s, axes=(-2, -1): torch.fft.irfft2(x, s=s, dim=axes).to(torch.bfloat16))(y_f, (1, L), axes=(1, 2))[:, 0, :S, :]
        y = y + kv * ssm_D[None, None, :]
        return q * y

    def _B_ref(x, w1, w2, w3, w4):
        B, S, D = x.shape
        h = _gelu(_layernorm(x, axis=-1) @ w1)
        c = D // 4
        g0 = _pad(h[:, :, 0 * c:1 * c], ((0, 0), (1, 0), (0, 0)))[:, :S, :]
        g1 = _pad(h[:, :, 1 * c:2 * c], ((0, 0), (0, 1), (0, 0)))[:, 1:, :]
        g2 = _pad(h[:, :, 2 * c:3 * c], ((0, 0), (2, 0), (0, 0)))[:, :S, :]
        g3 = _pad(h[:, :, 3 * c:], ((0, 0), (0, 2), (0, 0)))[:, 2:, :]
        hs = torch.concatenate([g0, g1, g2, g3], axis=-1) @ w2
        x1 = x + hs
        h2 = _gelu(_layernorm(x1, axis=-1) @ w3) @ w4
        return x1 + h2
    x = A_q
    mod = (_silu(c) @ w_mod).reshape(c.shape[0], 6, 1, -1)
    sa, ca, ga, sb, cb, gb = (mod[:, 0].clone(), mod[:, 1].clone(), mod[:, 2].clone(), mod[:, 3].clone(), mod[:, 4].clone(), mod[:, 5].clone())
    h = _layernorm(x, axis=-1) * (1 + ca) + sa
    x = x + ga * _A_ref(h, A_k, A_v, A_shift_k, A_ssm_k, A_ssm_D)
    h = _layernorm(x, axis=-1) * (1 + cb) + sb
    x = x + gb * _B_ref(h, B_w1, B_w2, B_w3, B_w4)
    return x
```

```cpp
#include <hip/hip_runtime.h>
#include <hip/hip_bf16.h>
typedef unsigned short u16;
constexpr int S = 2048, D = 512, F = 1792;
__device__ __forceinline__ float b2f(u16 x) { return __uint_as_float(((unsigned)x) << 16); }
__device__ __forceinline__ u16 f2b(float f) { __hip_bfloat16 h = __float2bfloat16(f); return *(u16*)&h; }
__device__ __forceinline__ float rb(float f) { return b2f(f2b(f)); }
__device__ __forceinline__ float gelu_t(float x) { float u = 0.7978845608028654f * (x + 0.044715f * x * x * x); return 0.5f * x * (1.f + tanhf(u)); }

__global__ void k_mod(const u16* c, const u16* wm, float* mod) {
  int n = blockIdx.x * blockDim.x + threadIdx.x;
  float acc = 0;
  for (int k = 0; k < D; ++k) { float x = b2f(c[k]); float s = rb(x / (1.f + __expf(-x))); acc += s * b2f(wm[k * 3072 + n]); }
  mod[n] = rb(acc);
}
__global__ void k_kv(const u16* k, const u16* v, const u16* sh, u16* kvT) {
  int i = blockIdx.x * blockDim.x + threadIdx.x; int s = i / D, d = i % D;
  float a = 0;
  for (int t = 0; t < 4; ++t) { int ss = s + t - 3; float kk = ss >= 0 ? b2f(k[ss * D + d]) : 0.f; a = rb(a + rb(kk * b2f(sh[d * 4 + 3 - t]))); }
  kvT[d * S + s] = f2b(a * b2f(v[i]));
}
__global__ void k_conv(const u16* kvT, const u16* sk, const u16* sD, u16* z) {
  __shared__ float u[S], h[S];
  int d = blockIdx.x;
  for (int i = threadIdx.x; i < S; i += blockDim.x) { u[i] = b2f(kvT[d * S + i]); h[i] = b2f(sk[d * S + i]); }
  __syncthreads();
  for (int s = threadIdx.x; s < S; s += blockDim.x) {
    float acc = 0;
    for (int j = 0; j <= s; ++j) acc += h[j] * u[s - j];
    z[s * D + d] = f2b(rb(acc) + rb(u[s] * b2f(sD[d])));
  }
}
__device__ float blk_sum(float v, float* red) {
  for (int o = 32; o; o >>= 1) v += __shfl_xor(v, o);
  __syncthreads();
  if ((threadIdx.x & 63) == 0) red[threadIdx.x >> 6] = v;
  __syncthreads();
  float r = 0; for (int i = 0; i < 8; ++i) r += red[i];
  return r;
}
__device__ float ln(float x, float* red) {
  float m = blk_sum(x, red) / D; float dd = x - m; float var = blk_sum(dd * dd, red) / D;
  return dd * rsqrtf(var + 1e-5f);
}
__global__ void k_ln1(const u16* q, const u16* z, const float* mod, u16* x1, u16* h2, u16* a1) {
  __shared__ float red[8];
  int s = blockIdx.x, d = threadIdx.x;
  float x = b2f(q[s * D + d]);
  float h = ln(x, red) * (1.f + mod[D + d]) + mod[d];
  float xx = rb(x + mod[2 * D + d] * (h * b2f(z[s * D + d])));
  x1[s * D + d] = f2b(xx);
  float hh = rb(ln(xx, red) * (1.f + mod[4 * D + d]) + mod[3 * D + d]);
  h2[s * D + d] = f2b(hh);
  a1[s * D + d] = f2b(ln(hh, red));
}
template <int MODE>
__global__ void k_gemm(const u16* A, const u16* W, int K, int N, u16* C, const u16* r1, const u16* r2, const float* mod) {
  int n = blockIdx.x * blockDim.x + threadIdx.x, m = blockIdx.y;
  float acc = 0;
  if (MODE == 1) {
    for (int k = 0; k < K; ++k) { int g = k >> 7; int dl = g == 0 ? -1 : g == 1 ? 1 : g == 2 ? -2 : 2; int mm = m + dl; float a = (mm >= 0 && mm < S) ? b2f(A[mm * K + k]) : 0.f; acc += a * b2f(W[k * N + n]); }
  } else for (int k = 0; k < K; ++k) acc += b2f(A[m * K + k]) * b2f(W[k * N + n]);
  if (MODE == 0 || MODE == 2) C[m * N + n] = f2b(gelu_t(rb(acc)));
  else if (MODE == 1) C[m * N + n] = f2b(b2f(r1[m * N + n]) + rb(acc));
  else C[m * N + n] = f2b(b2f(r2[m * N + n]) + mod[5 * D + n] * (b2f(r1[m * N + n]) + rb(acc)));
}
__global__ void k_ln2(const u16* x, u16* a) {
  __shared__ float red[8];
  int s = blockIdx.x, d = threadIdx.x;
  a[s * D + d] = f2b(ln(b2f(x[s * D + d]), red));
}
extern "C" void kernel_launch(void* const* d_in, const int* in_sizes, int n_in, void* d_out, int out_size, void* d_ws, size_t ws_size, hipStream_t stream) {
  const u16 *q = (const u16*)d_in[0], *k = (const u16*)d_in[1], *v = (const u16*)d_in[2], *sh = (const u16*)d_in[3], *sk = (const u16*)d_in[4], *sD = (const u16*)d_in[5];
  const u16 *w1 = (const u16*)d_in[7], *w2 = (const u16*)d_in[8], *w3 = (const u16*)d_in[9], *w4 = (const u16*)d_in[10], *c = (const u16*)d_in[11], *wm = (const u16*)d_in[12];
  char* ws = (char*)d_ws; size_t MB = 1 << 20;
  float* mod = (float*)ws; u16 *kvT = (u16*)(ws + 1 * MB), *z = (u16*)(ws + 3 * MB), *x1 = (u16*)(ws + 5 * MB), *h2 = (u16*)(ws + 7 * MB), *a1 = (u16*)(ws + 9 * MB), *G = (u16*)(ws + 11 * MB), *x1p = (u16*)(ws + 13 * MB), *a3 = (u16*)(ws + 15 * MB), *H = (u16*)(ws + 17 * MB);
  k_mod<<<3072 / 64, 64, 0, stream>>>(c, wm, mod);
  k_kv<<<S * D / 256, 256, 0, stream>>>(k, v, sh, kvT);
  k_conv<<<D, 256, 0, stream>>>(kvT, sk, sD, z);
  k_ln1<<<S, 512, 0, stream>>>(q, z, mod, x1, h2, a1);
  k_gemm<0><<<dim3(D / 256, S), 256, 0, stream>>>(a1, w1, D, D, G, nullptr, nullptr, mod);
  k_gemm<1><<<dim3(D / 256, S), 256, 0, stream>>>(G, w2, D, D, x1p, h2, nullptr, mod);
  k_ln2<<<S, 512, 0, stream>>>(x1p, a3);
  k_gemm<2><<<dim3(F / 256, S), 256, 0, stream>>>(a3, w3, D, F, H, nullptr, nullptr, mod);
  k_gemm<3><<<dim3(D / 256, S), 256, 0, stream>>>(H, w4, F, D, (u16*)d_out, x1p, x1, mod);
}
```

```cpp
#include <hip/hip_runtime.h>
#include <stdint.h>

typedef unsigned short u16;
typedef unsigned int u32;
typedef u32 v4u __attribute__((ext_vector_type(4)));
typedef u32 v2u __attribute__((ext_vector_type(2)));
typedef float v2f __attribute__((ext_vector_type(2)));
typedef float v4f __attribute__((ext_vector_type(4)));
typedef float v16f __attribute__((ext_vector_type(16)));
typedef __bf16 bf8 __attribute__((ext_vector_type(8)));
typedef __bf16 bf2 __attribute__((ext_vector_type(2)));
#define DI __device__ __forceinline__
#define LAS __attribute__((address_space(3)))

constexpr int S = 2048, D = 512, F = 1792;
constexpr size_t MB = 1u << 20;
constexpr size_t OFF_KVT = 0;
constexpr size_t OFF_Z = 2 * MB;
constexpr size_t OFF_X1 = 4 * MB;
constexpr size_t OFF_H2 = 6 * MB;
constexpr size_t OFF_G = 8 * MB;
constexpr size_t OFF_X1P = 10 * MB;
constexpr size_t OFF_H = 12 * MB;
constexpr size_t OFF_W1 = 20 * MB;
constexpr size_t OFF_W2 = OFF_W1 + 512 * 1024;
constexpr size_t OFF_W3 = 21 * MB;
constexpr size_t OFF_W4 = 23 * MB;
constexpr size_t OFF_MOD = 25 * MB;
constexpr size_t OFF_QST = OFF_MOD + 16384;
constexpr size_t OFF_CS3 = OFF_QST + 16384;
constexpr size_t OFF_ST4 = OFF_CS3 + 32768;

DI float lo2f(u32 x) { return __uint_as_float(x << 16); }
DI float hi2f(u32 x) { return __uint_as_float(x & 0xffff0000u); }
DI u32 pk2(float lo, float hi) { v2f t = {lo, hi}; bf2 b = __builtin_convertvector(t, bf2); return __builtin_bit_cast(u32, b); }
DI float b2f(u16 x) { return __uint_as_float(((u32)x) << 16); }
DI v16f mfma32(v4u a, v4u b, v16f c) { return __builtin_amdgcn_mfma_f32_32x32x16_bf16(__builtin_bit_cast(bf8, a), __builtin_bit_cast(bf8, b), c, 0, 0, 0); }
DI v4f mfma16(v4u a, v4u b, v4f c) { return __builtin_amdgcn_mfma_f32_16x16x32_bf16(__builtin_bit_cast(bf8, a), __builtin_bit_cast(bf8, b), c, 0, 0, 0); }
template <int CTRL> DI float dpp(float x) { return __builtin_bit_cast(float, __builtin_amdgcn_mov_dpp(__builtin_bit_cast(int, x), CTRL, 0xf, 0xf, true)); }
DI float red8(float x) { x += dpp<0xB1>(x); x += dpp<0x4E>(x); x += dpp<0x141>(x); return x; }
DI float swap32sum(float x) { auto r = __builtin_amdgcn_permlane32_swap(__float_as_uint(x), __float_as_uint(x), false, false); return __uint_as_float(r[0]) + __uint_as_float(r[1]); }
DI float gelu_t(float x) {
  const float c1 = -2.302208198f, c2 = -0.1029432397f;
  float e = __builtin_amdgcn_exp2f(x * __builtin_fmaf(x * x, c2, c1));
  return x * __builtin_amdgcn_rcpf(1.f + e);
}
DI int wave_id() { return __builtin_amdgcn_readfirstlane(threadIdx.x >> 6); }

__global__ __launch_bounds__(256) void k0_kvt(const u16* __restrict__ k, const u16* __restrict__ v, const u16* __restrict__ sh, char* __restrict__ ws) {
  const int b = blockIdx.x, tid = threadIdx.x;
  const int g = b & 7, i = b >> 3, cg = tid & 7, rp = tid >> 3;
  const int r = 64 * i + 2 * rp, d0 = 64 * g + 8 * cg;
  v4u kk[5], vv[2], sc[4];
#pragma unroll
  for (int j = 0; j < 5; ++j) { int rr = r - 3 + j; kk[j] = rr >= 0 ? *(const v4u*)(k + rr * D + d0) : (v4u){0, 0, 0, 0}; }
#pragma unroll
  for (int j = 0; j < 2; ++j) vv[j] = *(const v4u*)(v + (r + j) * D + d0);
#pragma unroll
  for (int j = 0; j < 4; ++j) sc[j] = *(const v4u*)(sh + d0 * 4 + 8 * j);
  u32* out = (u32*)(ws + OFF_KVT) + (size_t)d0 * (S / 2) + (2046 - r) / 2;
#pragma unroll
  for (int c = 0; c < 8; ++c) {
    float s0 = lo2f(sc[c >> 1][(c & 1) * 2]), s1 = hi2f(sc[c >> 1][(c & 1) * 2]), s2 = lo2f(sc[c >> 1][(c & 1) * 2 + 1]), s3 = hi2f(sc[c >> 1][(c & 1) * 2 + 1]);
    float kf[5], vf[2];
#pragma unroll
    for (int j = 0; j < 5; ++j) kf[j] = (c & 1) ? hi2f(kk[j][c >> 1]) : lo2f(kk[j][c >> 1]);
#pragma unroll
    for (int j = 0; j < 2; ++j) vf[j] = (c & 1) ? hi2f(vv[j][c >> 1]) : lo2f(vv[j][c >> 1]);
    float a0 = kf[0] * s3 + kf[1] * s2 + kf[2] * s1 + kf[3] * s0;
    float a1 = kf[1] * s3 + kf[2] * s2 + kf[3] * s1 + kf[4] * s0;
    out[c * (S / 2)] = pk2(a1 * vf[1], a0 * vf[0]);
  }
}

constexpr int HC_STRIDE = 4160;
constexpr int HC_BYTES = 2 * 4 * HC_STRIDE;
constexpr int U_STRIDE = 2304 * 2;
constexpr int K1_LDS = HC_BYTES + 2 * U_STRIDE;
constexpr int N_TILE_UNITS = 576, N_MOD_WG = 48, N_TILE_WG = N_TILE_UNITS / 8, N_QST_WG = 32;
constexpr int K1_GRID = 256 + N_MOD_WG + N_TILE_WG + N_QST_WG;

struct K1Args { const u16 *q, *sk, *sD, *w1, *w2, *w3, *w4, *c, *wm; char* ws; };

DI void conv_block(const K1Args& a, char* lds) {
  const int b = blockIdx.x, tid = threadIdx.x, lane = tid & 63, w = wave_id();
  const int d0 = 64 * (b & 7) + 2 * (b >> 3);
  {
    const int ch = tid >> 8, t = tid & 255;
    v4u uu = *(const v4u*)(a.ws + OFF_KVT + (size_t)(d0 + ch) * 4096 + t * 16);
    const v4u* hp = (const v4u*)(a.sk + (size_t)(d0 + ch) * S);
    v4u P = hp[t], Q = t < 255 ? hp[t + 1] : (v4u){0, 0, 0, 0};
    *(v4u*)(lds + HC_BYTES + ch * U_STRIDE + t * 16) = uu;
    if (t < 32) *(v4u*)(lds + HC_BYTES + ch * U_STRIDE + 4096 + t * 16) = (v4u){0, 0, 0, 0};
    u32 a0 = __builtin_amdgcn_alignbit(P[1], P[0], 16), a1 = __builtin_amdgcn_alignbit(P[2], P[1], 16), a2 = __builtin_amdgcn_alignbit(P[3], P[2], 16),
        a3 = __builtin_amdgcn_alignbit(Q[0], P[3], 16), a4 = __builtin_amdgcn_alignbit(Q[1], Q[0], 16);
    char* hc = lds + ch * 4 * HC_STRIDE + (t + 2) * 16;
    *(v4u*)(hc + 0 * HC_STRIDE) = (v4u){a0, a1, a2, a3};
    *(v4u*)(hc + 1 * HC_STRIDE) = (v4u){P[1], P[2], P[3], Q[0]};
    *(v4u*)(hc + 2 * HC_STRIDE) = (v4u){a1, a2, a3, a4};
    *(v4u*)(hc + 3 * HC_STRIDE) = (v4u){P[2], P[3], Q[0], Q[1]};
    if (t == 0) {
      char* h0 = lds + ch * 4 * HC_STRIDE;
      const v4u z = {0, 0, 0, 0};
#pragma unroll
      for (int c = 0; c < 4; ++c) *(v4u*)(h0 + c * HC_STRIDE) = z;
      *(v4u*)(h0 + 16 + 0 * HC_STRIDE) = (v4u){0, 0, 0, P[0] << 16};
      *(v4u*)(h0 + 16 + 1 * HC_STRIDE) = (v4u){0, 0, 0, P[0]};
      *(v4u*)(h0 + 16 + 2 * HC_STRIDE) = (v4u){0, 0, P[0] << 16, a0};
      *(v4u*)(h0 + 16 + 3 * HC_STRIDE) = (v4u){0, 0, P[0], P[1]};
    }
  }
  __syncthreads();
  const int ch = w & 1, pset = (w >> 1) & 1, lh = w >> 2;
  const int m = lane & 15, kg = lane >> 4;
  const LAS char* hbase = (const LAS char*)lds + ch * 4 * HC_STRIDE + (m & 3) * HC_STRIDE + 2 * (8 * kg + (m & 12)) + lh * 4 * 64;
  const LAS char* ubase = (const LAS char*)lds + HC_BYTES + ch * U_STRIDE + 2 * (2032 - 16 * m + 8 * kg) + lh * 4 * 64;
  v4f acc[4] = {};
#pragma unroll 1
  for (int lp = 0; lp < 4; ++lp) {
    v4u A[8], B[8];
#pragma unroll
    for (int j = 0; j < 8; ++j) {
      v2u x0 = *(const volatile LAS v2u*)(hbase + lp * 64 + j * 512), x1 = *(const volatile LAS v2u*)(hbase + lp * 64 + j * 512 + 8);
      A[j] = (v4u){x0[0], x0[1], x1[0], x1[1]};
      B[j] = *(const LAS v4u*)(ubase + lp * 64 - j * 512);
    }
    if (pset == 0) {
      acc[0] = mfma16(A[0], B[0], acc[0]);
#pragma unroll
      for (int j = 0; j <= 3; ++j) acc[1] = mfma16(A[j], B[3 - j], acc[1]);
#pragma unroll
      for (int j = 0; j <= 4; ++j) acc[2] = mfma16(A[j], B[4 - j], acc[2]);
#pragma unroll
      for (int j = 0; j <= 7; ++j) acc[3] = mfma16(A[j], B[7 - j], acc[3]);
    } else {
#pragma unroll
      for (int j = 0; j <= 1; ++j) acc[0] = mfma16(A[j], B[1 - j], acc[0]);
#pragma unroll
      for (int j = 0; j <= 2; ++j) acc[1] = mfma16(A[j], B[2 - j], acc[1]);
#pragma unroll
      for (int j = 0; j <= 5; ++j) acc[2] = mfma16(A[j], B[5 - j], acc[2]);
#pragma unroll
      for (int j = 0; j <= 6; ++j) acc[3] = mfma16(A[j], B[6 - j], acc[3]);
    }
  }
  __syncthreads();
  {
    float* red = (float*)lds + (lh * 2 + ch) * 2048;
#pragma unroll
    for (int i = 0; i < 4; ++i) {
      const int p = pset == 0 ? (i == 0 ? 0 : i == 1 ? 3 : i == 2 ? 4 : 7) : (i == 0 ? 1 : i == 1 ? 2 : i == 2 ? 5 : 6);
      *(v4f*)(red + 256 * p + 16 * m + 4 * kg) = acc[i];
    }
  }
  __syncthreads();
  {
    const float D0 = b2f(a.sD[d0]), D1 = b2f(a.sD[d0 + 1]);
    const float* red = (const float*)lds;
    const u16* u0 = (const u16*)(lds + HC_BYTES);
    const u16* u1 = (const u16*)(lds + HC_BYTES + U_STRIDE);
    char* zb = a.ws + OFF_Z + (d0 >> 3) * 128 + (d0 & 7) * 2;
#pragma unroll
    for (int i = 0; i < 4; ++i) {
      const int t = tid + 512 * i;
      float y0 = red[t] + red[2 * 2048 + t] + D0 * b2f(u0[2047 - t]);
      float y1 = red[2048 + t] + red[3 * 2048 + t] + D1 * b2f(u1[2047 - t]);
      *(u32*)(zb + (t >> 3) * 8192 + (t & 7) * 16) = pk2(y0, y1);
    }
  }
}

DI void mod_block(const K1Args& a, char* lds, int mb) {
  const int tid = threadIdx.x, lane = tid & 63, w = wave_id();
  const int r8 = lane >> 3, cg = lane & 7;
  v4u x[8]; float sv[8];
#pragma unroll
  for (int i = 0; i < 8; ++i) { const int kk = 64 * w + 8 * i + r8; x[i] = *(const v4u*)(a.wm + (size_t)kk * 3072 + 64 * mb + 8 * cg); }
#pragma unroll
  for (int i = 0; i < 8; ++i) { const int kk = 64 * w + 8 * i + r8; float c = b2f(a.c[kk]); float s = c * __builtin_amdgcn_rcpf(1.f + __builtin_amdgcn_exp2f(-1.442695041f * c)); sv[i] = lo2f(pk2(s, s)); }
  float acc[8] = {};
#pragma unroll
  for (int i = 0; i < 8; ++i)
#pragma unroll
    for (int j = 0; j < 4; ++j) { acc[2 * j] += sv[i] * lo2f(x[i][j]); acc[2 * j + 1] += sv[i] * hi2f(x[i][j]); }
  float* red = (float*)lds;
  *(v4f*)(red + (8 * w + r8) * 64 + 8 * cg) = (v4f){acc[0], acc[1], acc[2], acc[3]};
  *(v4f*)(red + (8 * w + r8) * 64 + 8 * cg + 4) = (v4f){acc[4], acc[5], acc[6], acc[7]};
  __syncthreads();
  if (tid < 64) {
    float s = 0;
#pragma unroll
    for (int r = 0; r < 64; ++r) s += red[r * 64 + tid];
    ((float*)(a.ws + OFF_MOD))[64 * mb + tid] = lo2f(pk2(s, s));
  }
}

DI void tile_unit(const K1Args& a, int u) {
  const int lane = threadIdx.x & 63, m = lane & 31, h = lane >> 5;
  const u16* W; char* dst; int N, nt, kc; bool nat = false, cs = false; int nks;
  if (u < 64) { W = a.w1; dst = a.ws + OFF_W1; N = D; nt = u >> 2; kc = u & 3; nat = true; nks = 32; }
  else if (u < 128) { W = a.w2; dst = a.ws + OFF_W2; N = D; nt = (u - 64) >> 2; kc = u & 3; nks = 32; }
  else if (u < 352) { W = a.w3; dst = a.ws + OFF_W3; N = F; nt = (u - 128) >> 2; kc = u & 3; cs = true; nks = 32; }
  else { W = a.w4; dst = a.ws + OFF_W4; N = D; nt = (u - 352) / 14; kc = (u - 352) % 14; nks = 112; }
  const u16* src = W + (size_t)(128 * kc + (nat ? 8 * h : 4 * h)) * N + 32 * nt + m;
  const int hs = nat ? 4 : 8;
  u16 e[8][8];
#pragma unroll
  for (int s = 0; s < 8; ++s)
#pragma unroll
    for (int j = 0; j < 8; ++j) e[s][j] = src[(size_t)(16 * s + hs * (j >> 2) + (j & 3)) * N];
  float sum = 0;
#pragma unroll
  for (int s = 0; s < 8; ++s) {
    v4u o;
#pragma unroll
    for (int j = 0; j < 4; ++j) { o[j] = (u32)e[s][2 * j] | ((u32)e[s][2 * j + 1] << 16); sum += b2f(e[s][2 * j]) + b2f(e[s][2 * j + 1]); }
    *(v4u*)(dst + ((size_t)nt * nks + 8 * kc + s) * 1024 + lane * 16) = o;
  }
  if (cs) { sum = swap32sum(sum); if (h == 0) ((float*)(a.ws + OFF_CS3))[kc * F + 32 * nt + m] = sum; }
}

DI void qstat_unit(const K1Args& a, int u) {
  const int lane = threadIdx.x & 63, t8 = lane >> 3, fg = lane & 7;
  const int tok = 8 * u + t8;
  v4u x[8];
#pragma unroll
  for (int c = 0; c < 8; ++c) x[c] = *(const v4u*)(a.q + tok * D + 64 * c + 8 * fg);
  float s = 0;
#pragma unroll
  for (int c = 0; c < 8; ++c)
#pragma unroll
    for (int j = 0; j < 4; ++j) s += lo2f(x[c][j]) + hi2f(x[c][j]);
  const float mean = red8(s) * (1.f / D);
  float ss = 0;
#pragma unroll
  for (int c = 0; c < 8; ++c)
#pragma unroll
    for (int j = 0; j < 4; ++j) { float p = lo2f(x[c][j]) - mean, q2 = hi2f(x[c][j]) - mean; ss += p * p + q2 * q2; }
  const float r = __builtin_amdgcn_rsqf(red8(ss) * (1.f / D) + 1e-5f);
  if (fg == 0) ((v2f*)(a.ws + OFF_QST))[tok] = (v2f){r, -mean * r};
}

__global__ __launch_bounds__(512, 4) void k1_conv(K1Args a) {
  __shared__ __attribute__((aligned(16))) char lds[K1_LDS];
  const int b = blockIdx.x;
  if (b < 256) conv_block(a, lds);
  else if (b < 256 + N_MOD_WG) mod_block(a, lds, b - 256);
  else if (b < 256 + N_MOD_WG + N_TILE_WG) tile_unit(a, (b - 256 - N_MOD_WG) * 8 + wave_id());
  else qstat_unit(a, (b - 256 - N_MOD_WG - N_TILE_WG) * 8 + wave_id());
}

__global__ __launch_bounds__(256, 1) void p1_kernel(const u16* __restrict__ q, char* __restrict__ ws) {
  __shared__ __attribute__((aligned(16))) char lds[32768 + 8192];
  const int b = blockIdx.x, tid = threadIdx.x, lane = tid & 63, w = wave_id();
  const int idx = b >> 3, tt = 8 * (b & 7) + (idx >> 2), cgp = idx & 3;
  const int t8 = lane >> 3, fg = lane & 7, n = 8 * w + t8, tok = 32 * tt + n;
  v4u qv[8], zv[8], wf[32];
#pragma unroll
  for (int ci = 0; ci < 8; ++ci) {
    const int c = (2 * cgp + ci) & 7;
    qv[ci] = *(const v4u*)(q + tok * D + 64 * c + 8 * fg);
    zv[ci] = *(const v4u*)(ws + OFF_Z + (tok >> 3) * 8192 + (8 * c + fg) * 128 + t8 * 16);
  }
  const v2f qs = ((const v2f*)(ws + OFF_QST))[tok];
  {
    const float* mod = (const float*)(ws + OFF_MOD);
    float* V = (float*)(lds + 32768);
#pragma unroll
    for (int i = 0; i < 2; ++i) {
      const int f = tid + 256 * i;
      const float sa = mod[f], ca = mod[D + f], ga = mod[2 * D + f], sb = mod[3 * D + f], cb = mod[4 * D + f];
      V[f] = ga * (1.f + ca); V[D + f] = ga * sa; V[2 * D + f] = 1.f + cb; V[3 * D + f] = sb;
    }
  }
  const char* wsrc = ws + OFF_W1 + (size_t)(4 * cgp + w) * 32768 + lane * 16;
#pragma unroll
  for (int s = 0; s < 32; ++s) wf[s] = *(const v4u*)(wsrc + s * 1024);
  __syncthreads();
  const float* V = (const float*)(lds + 32768);
  float x[64];
  float s1 = 0, s2 = 0;
#pragma unroll
  for (int ci = 0; ci < 8; ++ci) {
    const int f0 = 64 * ((2 * cgp + ci) & 7) + 8 * fg;
    const v4f A0 = *(const v4f*)(V + f0), A1 = *(const v4f*)(V + f0 + 4), B0 = *(const v4f*)(V + D + f0), B1 = *(const v4f*)(V + D + f0 + 4);
#pragma unroll
    for (int j = 0; j < 8; ++j) {
      const float qq = (j & 1) ? hi2f(qv[ci][j >> 1]) : lo2f(qv[ci][j >> 1]);
      const float zz = (j & 1) ? hi2f(zv[ci][j >> 1]) : lo2f(zv[ci][j >> 1]);
      const float Aj = j < 4 ? A0[j & 3] : A1[j & 3], Bj = j < 4 ? B0[j & 3] : B1[j & 3];
      const float t = qq * qs[0] + qs[1];
      const float xx = (t * Aj + Bj) * zz + qq;
      x[8 * ci + j] = xx; s1 += xx; s2 += xx * xx;
    }
  }
  const size_t afo = (size_t)tt * 32768 + (size_t)(8 * cgp + (fg >> 1)) * 1024 + n * 16 + (fg & 1) * 8;
#pragma unroll
  for (int ci = 0; ci < 2; ++ci)
#pragma unroll
    for (int hh = 0; hh < 2; ++hh)
      *(v2u*)(ws + OFF_X1 + afo + ci * 4096 + hh * 512) = (v2u){pk2(x[8 * ci + 4 * hh], x[8 * ci + 4 * hh + 1]), pk2(x[8 * ci + 4 * hh + 2], x[8 * ci + 4 * hh + 3])};
  s1 = red8(s1) * (1.f / D); s2 = red8(s2) * (1.f / D);
  float r2 = __builtin_amdgcn_rsqf(fmaxf(s2 - s1 * s1, 0.f) + 1e-5f), o2 = -s1 * r2;
  s1 = 0; s2 = 0;
#pragma unroll
  for (int ci = 0; ci < 8; ++ci) {
    const int f0 = 64 * ((2 * cgp + ci) & 7) + 8 * fg;
    const v4f C0 = *(const v4f*)(V + 2 * D + f0), C1 = *(const v4f*)(V + 2 * D + f0 + 4), E0 = *(const v4f*)(V + 3 * D + f0), E1 = *(const v4f*)(V + 3 * D + f0 + 4);
#pragma unroll
    for (int j = 0; j < 8; ++j) {
      const float Cj = j < 4 ? C0[j & 3] : C1[j & 3], Ej = j < 4 ? E0[j & 3] : E1[j & 3];
      const float hh = (x[8 * ci + j] * r2 + o2) * Cj + Ej;
      x[8 * ci + j] = hh; s1 += hh; s2 += hh * hh;
    }
  }
#pragma unroll
  for (int ci = 0; ci < 2; ++ci)
#pragma unroll
    for (int hh = 0; hh < 2; ++hh)
      *(v2u*)(ws + OFF_H2 + afo + ci * 4096 + hh * 512) = (v2u){pk2(x[8 * ci + 4 * hh], x[8 * ci + 4 * hh + 1]), pk2(x[8 * ci + 4 * hh + 2], x[8 * ci + 4 * hh + 3])};
  s1 = red8(s1) * (1.f / D); s2 = red8(s2) * (1.f / D);
  const float r3 = __builtin_amdgcn_rsqf(fmaxf(s2 - s1 * s1, 0.f) + 1e-5f), o3 = -s1 * r3;
#pragma unroll
  for (int ci = 0; ci < 8; ++ci) {
    const int g8 = 8 * ((2 * cgp + ci) & 7) + fg;
    v4u o;
#pragma unroll
    for (int j = 0; j < 4; ++j) o[j] = pk2(x[8 * ci + 2 * j] * r3 + o3, x[8 * ci + 2 * j + 1] * r3 + o3);
    *(v4u*)(lds + (g8 >> 1) * 1024 + ((((g8 & 1) * 32 + n) ^ fg) * 16)) = o;
  }
  __syncthreads();
  v16f acc = {};
  const int h = lane >> 5;
#pragma unroll
  for (int s = 0; s < 32; ++s) {
    const v4u xf = *(const v4u*)(lds + s * 1024 + ((lane ^ ((2 * s + h) & 7)) * 16));
    acc = mfma32(wf[s], xf, acc);
  }
  char* gdst = ws + OFF_G + (size_t)tt * 32768 + (size_t)(2 * (4 * cgp + w)) * 1024 + lane * 16;
#pragma unroll
  for (int s = 0; s < 2; ++s) {
    v4u o;
#pragma unroll
    for (int j = 0; j < 4; ++j) o[j] = pk2(gelu_t(acc[8 * s + 2 * j]), gelu_t(acc[8 * s + 2 * j + 1]));
    *(v4u*)(gdst + s * 1024) = o;
  }
}

DI v16f reduce4(v16f (&acc)[4], char* lds, int w, int lane) {
#pragma unroll
  for (int l = 1; l < 4; ++l)
#pragma unroll
    for (int q = 0; q < 4; ++q)
      *(v4f*)(lds + ((l ^ w) * 3 + (l - 1)) * 4096 + q * 1024 + lane * 16) = (v4f){acc[l][4 * q], acc[l][4 * q + 1], acc[l][4 * q + 2], acc[l][4 * q + 3]};
  __syncthreads();
  v16f r = acc[0];
#pragma unroll
  for (int l = 0; l < 3; ++l)
#pragma unroll
    for (int q = 0; q < 4; ++q) {
      const v4f t = *(const v4f*)(lds + (w * 3 + l) * 4096 + q * 1024 + lane * 16);
      r[4 * q] += t[0]; r[4 * q + 1] += t[1]; r[4 * q + 2] += t[2]; r[4 * q + 3] += t[3];
    }
  return r;
}

__global__ __launch_bounds__(256, 1) void p2_kernel(char* __restrict__ ws) {
  __shared__ __attribute__((aligned(16))) char lds[49152];
  const int b = blockIdx.x, lane = threadIdx.x & 63, w = wave_id();
  const int idx = b >> 3, rg = 4 * (b & 7) + (idx >> 3), cj = idx & 7;
  const int n = lane & 31, h = lane >> 5;
  const int dl = w == 0 ? -1 : w == 1 ? 1 : w == 2 ? -2 : 2;
  const auto rG = __builtin_amdgcn_make_buffer_rsrc(ws + OFF_G, 0, 2 * MB, 0x00020000);
  v4u xf[2][8], wf[2][8], hf[2];
#pragma unroll
  for (int i = 0; i < 2; ++i) {
    const int t = 64 * rg + 32 * (i ^ (w >> 1)) + n + dl;
    const u32 off = (t >= 0 && t < S) ? (u32)((t >> 5) * 32768 + (h * 32 + (t & 31)) * 16) : 0x80000000u;
#pragma unroll
    for (int s = 0; s < 8; ++s) xf[i][s] = __builtin_amdgcn_raw_buffer_load_b128(rG, off + s * 1024, w * 8192, 0);
  }
#pragma unroll
  for (int i = 0; i < 2; ++i)
#pragma unroll
    for (int s = 0; s < 8; ++s) wf[i][s] = *(const v4u*)(ws + OFF_W2 + (size_t)(2 * cj + (i ^ (w & 1))) * 32768 + (8 * w + s) * 1024 + lane * 16);
  const int tt = 2 * rg + (w >> 1), nt = 2 * cj + (w & 1);
  const size_t afo = (size_t)tt * 32768 + (size_t)nt * 2048 + lane * 16;
#pragma unroll
  for (int s = 0; s < 2; ++s) hf[s] = *(const v4u*)(ws + OFF_H2 + afo + s * 1024);
  v16f acc[4] = {};
#pragma unroll
  for (int s = 0; s < 8; ++s)
#pragma unroll
    for (int l = 0; l < 4; ++l) acc[l] = mfma32(wf[l & 1][s], xf[l >> 1][s], acc[l]);
  v16f r = reduce4(acc, lds, w, lane);
  float s1 = 0, s2 = 0;
#pragma unroll
  for (int s = 0; s < 2; ++s) {
    v4u o;
#pragma unroll
    for (int j = 0; j < 4; ++j) {
      o[j] = pk2(r[8 * s + 2 * j] + lo2f(hf[s][j]), r[8 * s + 2 * j + 1] + hi2f(hf[s][j]));
      const float a0 = lo2f(o[j]), a1 = hi2f(o[j]);
      s1 += a0 + a1; s2 += a0 * a0 + a1 * a1;
    }
    *(v4u*)(ws + OFF_X1P + afo + s * 1024) = o;
  }
  s1 = swap32sum(s1); s2 = swap32sum(s2);
  if (h == 0) ((v2f*)(ws + OFF_ST4))[(tt * 16 + nt) * 32 + n] = (v2f){s1, s2};
}

__global__ __launch_bounds__(256, 1) void p3_kernel(char* __restrict__ ws) {
  __shared__ __attribute__((aligned(16))) char lds[65536];
  const int b = blockIdx.x, lane = threadIdx.x & 63, w = wave_id();
  const int idx = b >> 3, rg = 4 * (b & 7) + (idx >> 3), cj = idx & 7;
  const int n = lane & 31, h = lane >> 5;
  constexpr int PF = 20;
  v4u xs[16], wf[2][32];
  const char* xsrc = ws + OFF_X1P + (size_t)rg * 65536 + w * 16384 + lane * 16;
#pragma unroll
  for (int i = 0; i < 16; ++i) xs[i] = *(const v4u*)(xsrc + i * 1024);
  const int nt0 = 7 * cj + 2 * w;
  const int nt1 = w < 3 ? nt0 + 1 : nt0;
  const char* w0 = ws + OFF_W3 + (size_t)nt0 * 32768 + lane * 16;
  const char* w1 = ws + OFF_W3 + (size_t)nt1 * 32768 + lane * 16;
#pragma unroll
  for (int s = 0; s < PF; ++s) { wf[0][s] = *(const v4u*)(w0 + s * 1024); if (w < 3) wf[1][s] = *(const v4u*)(w1 + s * 1024); }
#pragma unroll
  for (int i = 0; i < 16; ++i) *(v4u*)(lds + w * 16384 + i * 1024 + lane * 16) = xs[i];
  float rs[2], mo[2];
#pragma unroll
  for (int i = 0; i < 2; ++i) {
    const v2f* st = (const v2f*)(ws + OFF_ST4) + ((2 * rg + i) * 16 + 8 * h) * 32 + n;
    float s1 = 0, s2 = 0;
#pragma unroll
    for (int j = 0; j < 8; ++j) { const v2f t = st[j * 32]; s1 += t[0]; s2 += t[1]; }
    s1 = swap32sum(s1) * (1.f / D); s2 = swap32sum(s2) * (1.f / D);
    rs[i] = __builtin_amdgcn_rsqf(fmaxf(s2 - s1 * s1, 0.f) + 1e-5f); mo[i] = -s1 * rs[i];
  }
  __syncthreads();
  v16f acc[2][2] = {};
  if (w < 3) {
#pragma unroll
    for (int s = 0; s < 32; ++s) {
      if (s + PF < 32) { wf[0][s + PF] = *(const v4u*)(w0 + (s + PF) * 1024); wf[1][s + PF] = *(const v4u*)(w1 + (s + PF) * 1024); }
      const v4u x0 = *(const v4u*)(lds + s * 1024 + lane * 16), x1 = *(const v4u*)(lds + 32768 + s * 1024 + lane * 16);
      acc[0][0] = mfma32(wf[0][s], x0, acc[0][0]); acc[0][1] = mfma32(wf[0][s], x1, acc[0][1]);
      acc[1][0] = mfma32(wf[1][s], x0, acc[1][0]); acc[1][1] = mfma32(wf[1][s], x1, acc[1][1]);
    }
  } else {
#pragma unroll
    for (int s = 0; s < 32; ++s) {
      if (s + PF < 32) wf[0][s + PF] = *(const v4u*)(w0 + (s + PF) * 1024);
      const v4u x0 = *(const v4u*)(lds + s * 1024 + lane * 16), x1 = *(const v4u*)(lds + 32768 + s * 1024 + lane * 16);
      acc[0][0] = mfma32(wf[0][s], x0, acc[0][0]); acc[0][1] = mfma32(wf[0][s], x1, acc[0][1]);
    }
  }
#pragma unroll
  for (int j = 0; j < 2; ++j) {
    if (j == 1 && w == 3) break;
    const int nt = nt0 + j;
    float cs[16];
#pragma unroll
    for (int q = 0; q < 4; ++q) {
      const float* cp = (const float*)(ws + OFF_CS3) + 32 * nt + 8 * q + 4 * h;
      const v4f t = *(const v4f*)cp + *(const v4f*)(cp + F) + *(const v4f*)(cp + 2 * F) + *(const v4f*)(cp + 3 * F);
      cs[4 * q] = t[0]; cs[4 * q + 1] = t[1]; cs[4 * q + 2] = t[2]; cs[4 * q + 3] = t[3];
    }
#pragma unroll
    for (int i = 0; i < 2; ++i) {
      char* dst = ws + OFF_H + (size_t)(2 * rg + i) * (112 * 1024) + (size_t)nt * 2048 + lane * 16;
#pragma unroll
      for (int s = 0; s < 2; ++s) {
        v4u o;
#pragma unroll
        for (int jj = 0; jj < 4; ++jj) {
          const int r0 = 8 * s + 2 * jj;
          o[jj] = pk2(gelu_t(acc[j][i][r0] * rs[i] + cs[r0] * mo[i]), gelu_t(acc[j][i][r0 + 1] * rs[i] + cs[r0 + 1] * mo[i]));
        }
        *(v4u*)(dst + s * 1024) = o;
      }
    }
  }
}

__global__ __launch_bounds__(256, 1) void p4_kernel(char* __restrict__ ws, u16* __restrict__ out) {
  __shared__ __attribute__((aligned(16))) char lds[49152];
  const int b = blockIdx.x, lane = threadIdx.x & 63, w = wave_id();
  const int idx = b >> 3, rg = 4 * (b & 7) + (idx >> 3), cj = idx & 7;
  const int n = lane & 31, h = lane >> 5;
  constexpr int PF = 14, NS = 28;
  v4u xf[2][NS], wf[2][NS];
  const char* xp[2]; const char* wp[2];
#pragma unroll
  for (int i = 0; i < 2; ++i) {
    xp[i] = ws + OFF_H + (size_t)(2 * rg + (i ^ (w >> 1))) * (112 * 1024) + (size_t)w * (NS * 1024) + lane * 16;
    wp[i] = ws + OFF_W4 + (size_t)(2 * cj + (i ^ (w & 1))) * (112 * 1024) + (size_t)w * (NS * 1024) + lane * 16;
  }
#pragma unroll
  for (int s = 0; s < PF; ++s)
#pragma unroll
    for (int i = 0; i < 2; ++i) { xf[i][s] = *(const v4u*)(xp[i] + s * 1024); wf[i][s] = *(const v4u*)(wp[i] + s * 1024); }
  v16f acc[4] = {};
#pragma unroll
  for (int s = 0; s < NS; ++s) {
    if (s + PF < NS) {
#pragma unroll
      for (int i = 0; i < 2; ++i) { xf[i][s + PF] = *(const v4u*)(xp[i] + (s + PF) * 1024); wf[i][s + PF] = *(const v4u*)(wp[i] + (s + PF) * 1024); }
    }
#pragma unroll
    for (int l = 0; l < 4; ++l) acc[l] = mfma32(wf[l & 1][s], xf[l >> 1][s], acc[l]);
    __builtin_amdgcn_sched_barrier(0);
  }
  const int tt = 2 * rg + (w >> 1), nt = 2 * cj + (w & 1);
  const size_t afo = (size_t)tt * 32768 + (size_t)nt * 2048 + lane * 16;
  v4u x1f[2], xpf[2]; v4f gb[4];
#pragma unroll
  for (int s = 0; s < 2; ++s) { x1f[s] = *(const v4u*)(ws + OFF_X1 + afo + s * 1024); xpf[s] = *(const v4u*)(ws + OFF_X1P + afo + s * 1024); }
#pragma unroll
  for (int q = 0; q < 4; ++q) gb[q] = *(const v4f*)((const float*)(ws + OFF_MOD) + 5 * D + 32 * nt + 8 * q + 4 * h);
  v16f r = reduce4(acc, lds, w, lane);
  u16* orow = out + (size_t)(32 * tt + n) * D + 32 * nt + 8 * h;
#pragma unroll
  for (int s = 0; s < 2; ++s) {
    u32 o[4];
#pragma unroll
    for (int j = 0; j < 4; ++j) {
      const int r0 = 8 * s + 2 * j;
      o[j] = pk2(lo2f(x1f[s][j]) + gb[r0 >> 2][r0 & 3] * (lo2f(xpf[s][j]) + r[r0]), hi2f(x1f[s][j]) + gb[r0 >> 2][(r0 & 3) + 1] * (hi2f(xpf[s][j]) + r[r0 + 1]));
    }
    auto e0 = __builtin_amdgcn_permlane32_swap(o[0], o[2], false, false);
    auto e1 = __builtin_amdgcn_permlane32_swap(o[1], o[3], false, false);
    *(v4u*)(orow + 16 * s) = (v4u){e0[0], e1[0], e0[1], e1[1]};
  }
}

extern "C" void kernel_launch(void* const* d_in, const int* in_sizes, int n_in, void* d_out, int out_size, void* d_ws, size_t ws_size, hipStream_t stream) {
  const u16 *q = (const u16*)d_in[0], *k = (const u16*)d_in[1], *v = (const u16*)d_in[2], *sh = (const u16*)d_in[3];
  char* ws = (char*)d_ws;
  K1Args a;
  a.q = q; a.sk = (const u16*)d_in[4]; a.sD = (const u16*)d_in[5]; a.w1 = (const u16*)d_in[7]; a.w2 = (const u16*)d_in[8]; a.w3 = (const u16*)d_in[9];
  a.w4 = (const u16*)d_in[10]; a.c = (const u16*)d_in[11]; a.wm = (const u16*)d_in[12]; a.ws = ws;
  k0_kvt<<<256, 256, 0, stream>>>(k, v, sh, ws);
  k1_conv<<<K1_GRID, 512, 0, stream>>>(a);
  p1_kernel<<<256, 256, 0, stream>>>(q, ws);
  p2_kernel<<<256, 256, 0, stream>>>(ws);
  p3_kernel<<<256, 256, 0, stream>>>(ws);
  p4_kernel<<<256, 256, 0, stream>>>(ws, (u16*)d_out);
}
```

```cpp
#include <hip/hip_runtime.h>
#include <stdint.h>

typedef unsigned short u16;
typedef unsigned int u32;
typedef unsigned long long u64;
typedef u32 v4u __attribute__((ext_vector_type(4)));
typedef u32 v2u __attribute__((ext_vector_type(2)));
typedef short v4s __attribute__((ext_vector_type(4)));
typedef float v2f __attribute__((ext_vector_type(2)));
typedef float v4f __attribute__((ext_vector_type(4)));
typedef float v16f __attribute__((ext_vector_type(16)));
typedef __bf16 bf8 __attribute__((ext_vector_type(8)));
typedef __bf16 bf2 __attribute__((ext_vector_type(2)));
#define DI __device__ __forceinline__
#define LAS __attribute__((address_space(3)))
#define GAS __attribute__((address_space(1)))
#define SB() __builtin_amdgcn_sched_barrier(0)
#define PIN(x) asm volatile("" : "+v"(x))
#define DRAIN() asm volatile("s_waitcnt vmcnt(0)" ::: "memory")
#define RLX_AGENT __ATOMIC_RELAXED, __HIP_MEMORY_SCOPE_AGENT

constexpr int S = 2048, D = 512, F = 1792;
constexpr u32 MB = 1u << 20;
constexpr u32 C_E = 0, C_FA = 1024, C_FW = 4096, C_CB = 8192, C_FG = 20480, C_FX = 24576, C_FH = 28672;
constexpr u32 OFF_KVT = 1 * MB;
constexpr u32 OFF_Z = 3 * MB;
constexpr u32 OFF_G = 5 * MB;
constexpr u32 OFF_X1 = 7 * MB;
constexpr u32 OFF_X1P = 9 * MB;
constexpr u32 OFF_H = 11 * MB;
constexpr u32 OFF_W1 = 18 * MB;
constexpr u32 OFF_W2 = OFF_W1 + 512 * 1024;
constexpr u32 OFF_W3 = 19 * MB;
constexpr u32 OFF_W4 = 21 * MB;
constexpr u32 OFF_MODP = 23 * MB;
constexpr u32 OFF_CSP = OFF_MODP + 32768;
constexpr u32 WS_BYTES = 24 * MB;

constexpr int HC_STRIDE = 4160;
constexpr int L_HC = 0;
constexpr int L_U = 33280;
constexpr int U_STRIDE = 4608;
constexpr int L_T = 43008;
constexpr int L_TILE = 51200;
constexpr int L_CRED = 75776;
constexpr int L_Q = 108544;
constexpr int L_X = 0;
constexpr int L_ZS = 65536;
constexpr int L_RED2 = 98304;
constexpr int L_RED4 = 0;
constexpr int L_XH = 131072;
constexpr int L_V = 147968;
constexpr int L_MISC = 158208;
constexpr int L_MODRED = L_MISC, L_CS = L_MISC + 1024, L_ST = L_MISC + 2048, L_CNT = L_MISC + 4096;
constexpr int LDS_BYTES = L_CNT + 16;

#define STAMP(slot) do {} while (0)
#define CSTAMP(slot) do {} while (0)

DI float lo2f(u32 x) { return __uint_as_float(x << 16); }
DI float hi2f(u32 x) { return __uint_as_float(x & 0xffff0000u); }
DI u32 pk2(float lo, float hi) { v2f t = {lo, hi}; bf2 b = __builtin_convertvector(t, bf2); return __builtin_bit_cast(u32, b); }
DI float b2f(u16 x) { return __uint_as_float(((u32)x) << 16); }
DI v16f mfma32(v4u a, v4u b, v16f c) { return __builtin_amdgcn_mfma_f32_32x32x16_bf16(__builtin_bit_cast(bf8, a), __builtin_bit_cast(bf8, b), c, 0, 0, 0); }
DI v4f mfma16(v4u a, v4u b, v4f c) { return __builtin_amdgcn_mfma_f32_16x16x32_bf16(__builtin_bit_cast(bf8, a), __builtin_bit_cast(bf8, b), c, 0, 0, 0); }
template <int CTRL> DI float dpp(float x) { return __builtin_bit_cast(float, __builtin_amdgcn_mov_dpp(__builtin_bit_cast(int, x), CTRL, 0xf, 0xf, true)); }
DI float red8(float x) { x += dpp<0xB1>(x); x += dpp<0x4E>(x); x += dpp<0x141>(x); return x; }
DI float tr32(float a, float b) { auto r = __builtin_amdgcn_permlane32_swap(__float_as_uint(a), __float_as_uint(b), false, false); return __uint_as_float(r[0]) + __uint_as_float(r[1]); }
DI float tr16(float a, float b) { auto r = __builtin_amdgcn_permlane16_swap(__float_as_uint(a), __float_as_uint(b), false, false); return __uint_as_float(r[0]) + __uint_as_float(r[1]); }
DI float dot2(u32 a, u32 b, float c) { return __builtin_amdgcn_fdot2_f32_bf16(__builtin_bit_cast(bf2, a), __builtin_bit_cast(bf2, b), c, false); }
DI float rdl(float x, int l) { return __builtin_bit_cast(float, __builtin_amdgcn_readlane(__builtin_bit_cast(int, x), l)); }
DI float gelu_t(float x) {
  const float c1 = -2.302208198f, c2 = -0.1029432397f;
  float e = __builtin_amdgcn_exp2f(x * __builtin_fmaf(x * x, c2, c1));
  return x * __builtin_amdgcn_rcpf(1.f + e);
}
typedef __amdgpu_buffer_rsrc_t rsrc_t;
DI rsrc_t mk_rsrc(const void* p, u32 bytes) { return __builtin_amdgcn_make_buffer_rsrc((void*)p, 0, bytes, 0x00020000); }
DI v4u bload(rsrc_t r, u32 voff, u32 soff) { return __builtin_amdgcn_raw_buffer_load_b128(r, voff, soff, 0); }
DI v4u cload(rsrc_t r, u32 voff, u32 soff) { return __builtin_amdgcn_raw_buffer_load_b128(r, voff, soff, 16); }
DI void cstore(v4u v, rsrc_t r, u32 voff) { __builtin_amdgcn_raw_buffer_store_b128(v, r, voff, 0, 16); }
DI void cstore1(float v, rsrc_t r, u32 voff) { __builtin_amdgcn_raw_buffer_store_b32(__float_as_uint(v), r, voff, 0, 16); }
DI float cload1(rsrc_t r, u32 voff, u32 soff) { return __uint_as_float(__builtin_amdgcn_raw_buffer_load_b32(r, voff, soff, 16)); }
DI void bar() { __syncthreads(); }

constexpr int SPIN_CAP = 1 << 17;
DI void poll(const char* ws, u32 off, bool active, u64 want) {
  GAS u64* p = (GAS u64*)(ws + off);
  for (int sp = 0; sp < SPIN_CAP; ++sp) {
    const u64 f = active ? __hip_atomic_load(p, RLX_AGENT) : want;
    if (__all(f == want)) break;
  }
}
DI void post(char* ws, u32 off, u64 tag) { __hip_atomic_store((GAS u64*)(ws + off), tag, RLX_AGENT); }

struct Args { const u16 *q, *k, *v, *sh, *sk, *sD, *w1, *w2, *w3, *w4, *c, *wm; u16* out; char* ws; };

DI void ln_stats(const float (&v)[8], int lane, float (&rs)[4], float (&mo)[4]) {
  float a0 = tr32(v[0], v[4]), a1 = tr32(v[1], v[5]), a2 = tr32(v[2], v[6]), a3 = tr32(v[3], v[7]);
  float s1 = tr16(a0, a2), s2 = tr16(a1, a3);
  s1 = red8(s1); s1 += dpp<0x140>(s1);
  s2 = red8(s2); s2 += dpp<0x140>(s2);
  s1 *= (1.f / D); s2 *= (1.f / D);
  const float r = __builtin_amdgcn_rsqf(fmaxf(s2 - s1 * s1, 0.f) + 1e-5f), o = -s1 * r;
#pragma unroll
  for (int t = 0; t < 4; ++t) { rs[t] = rdl(r, 16 * t); mo[t] = rdl(o, 16 * t); }
}

template <bool SWZ>
DI void gemm_small(const v4u (&wf)[16], char* lds, int w, int lane, v4f& m0, v4f& m1) {
  const int h = lane >> 5, kh = w >> 2;
  v16f acc = {};
  v4u xf[16];
#pragma unroll
  for (int s = 0; s < 16; ++s) xf[s] = *(const v4u*)(lds + L_X + kh * 16384 + s * 1024 + ((SWZ ? (lane ^ ((2 * s + h) & 7)) : lane) * 16));
  SB();
#pragma unroll
  for (int s = 0; s < 16; ++s) acc = mfma32(wf[s], xf[s], acc);
  PIN(acc);
  v4f g0, g1;
  if (kh == 0) { m0 = (v4f){acc[0], acc[1], acc[2], acc[3]}; m1 = (v4f){acc[4], acc[5], acc[6], acc[7]}; g0 = (v4f){acc[8], acc[9], acc[10], acc[11]}; g1 = (v4f){acc[12], acc[13], acc[14], acc[15]}; }
  else { g0 = (v4f){acc[0], acc[1], acc[2], acc[3]}; g1 = (v4f){acc[4], acc[5], acc[6], acc[7]}; m0 = (v4f){acc[8], acc[9], acc[10], acc[11]}; m1 = (v4f){acc[12], acc[13], acc[14], acc[15]}; }
  char* red = lds + L_RED2;
  *(v4f*)(red + (w ^ 4) * 2048 + lane * 16) = g0; *(v4f*)(red + (w ^ 4) * 2048 + 1024 + lane * 16) = g1;
  bar();
  m0 += *(const v4f*)(red + w * 2048 + lane * 16); m1 += *(const v4f*)(red + w * 2048 + 1024 + lane * 16);
}

template <bool MOD>
DI void prep(const Args& a, char* lds, const rsrc_t rws, int b, int tid, int lane, int w, int d0, int tt, u64& tag) {
  constexpr int NUN = MOD ? 1 : 3;
  const int t2 = tid - 256, ch = t2 >> 7, t = t2 & 127, wq = w - 4;
  const rsrc_t rh = mk_rsrc(a.sk, S * D * 2);
  const u32 ho = (u32)((d0 + ch) * 4096 + t * 16);
  v4u P[2], Q[2];
#pragma unroll
  for (int j = 0; j < 2; ++j) { P[j] = bload(rh, ho + j * 2048, 0); Q[j] = bload(rh, ho + j * 2048 + 16, 0); }
  SB();
  const int u0 = MOD ? 4 * b + wq : 384 + 12 * (b - 96) + 3 * wq;
  v4u tl[NUN][2], mx[8], qs[8]; u32 cc[8];
  u32 tdst[NUN], tstr[NUN]; int tcs[NUN];
#pragma unroll
  for (int j = 0; j < NUN; ++j) {
    const int u = u0 + j;
    const bool is1 = u < 256, is2 = u < 512, is3 = u < 1408;
    const u16* W = is1 ? a.w1 : is2 ? a.w2 : is3 ? a.w3 : a.w4;
    const bool w3 = !is2 && is3;
    const int N = w3 ? F : D;
    const int ur = is1 ? u : is2 ? u - 256 : is3 ? u - 512 : u - 1408;
    const int s = w3 ? ur / 28 : ur >> 3, np = w3 ? ur % 28 : ur & 7, nks = is3 ? 32 : 112;
    tdst[j] = (is1 ? OFF_W1 : is2 ? OFF_W2 : is3 ? OFF_W3 : OFF_W4) + (u32)(2 * np * nks + s) * 1024;
    tstr[j] = (u32)nks * 1024;
    tcs[j] = w3 ? s * F + 64 * np : -1;
    const rsrc_t rw = mk_rsrc(W, 0x7fffffff);
    const int jj = lane >> 3;
#pragma unroll
    for (int hh = 0; hh < 2; ++hh) {
      const int row = is1 ? 128 * (((np >> 1) + (jj >> 1)) & 3) + 2 * (2 * s + hh) + (jj & 1) : 16 * s + 8 * (jj >> 2) + 4 * hh + (jj & 3);
      tl[j][hh] = bload(rw, (u32)((row * N + 64 * np + 8 * (lane & 7)) * 2), 0);
    }
  }
  SB();
  const int mrow = 256 * (b & 1) + 64 * wq + (lane >> 3);
  if (MOD) {
    const rsrc_t rc = mk_rsrc(a.c, D * 2);
#pragma unroll
    for (int j = 0; j < 8; ++j) cc[j] = __builtin_amdgcn_raw_buffer_load_b16(rc, (u32)(mrow + 8 * j) * 2, 0, 0);
#pragma unroll
    for (int j = 0; j < 8; ++j) mx[j] = *(const v4u*)(a.wm + (size_t)(mrow + 8 * j) * 3072 + 64 * (b >> 1) + 8 * (lane & 7));
  }
  SB();
#pragma unroll
  for (int j = 0; j < 8; ++j) qs[j] = *(const v4u*)((const char*)(a.q + 32 * tt * D) + j * 4096 + t2 * 16);
  SB();
  asm volatile("" : "+s"(tag));
  tag += 1;
#pragma unroll
  for (int j = 0; j < 2; ++j) {
    const u32 a0 = __builtin_amdgcn_alignbit(P[j][1], P[j][0], 16), a1 = __builtin_amdgcn_alignbit(P[j][2], P[j][1], 16), a2 = __builtin_amdgcn_alignbit(P[j][3], P[j][2], 16),
              a3 = __builtin_amdgcn_alignbit(Q[j][0], P[j][3], 16), a4 = __builtin_amdgcn_alignbit(Q[j][1], Q[j][0], 16);
    char* hc = lds + L_HC + ch * 4 * HC_STRIDE + (t + 128 * j + 2) * 16;
    *(v4u*)(hc + 0 * HC_STRIDE) = (v4u){a0, a1, a2, a3};
    *(v4u*)(hc + 1 * HC_STRIDE) = (v4u){P[j][1], P[j][2], P[j][3], Q[j][0]};
    *(v4u*)(hc + 2 * HC_STRIDE) = (v4u){a1, a2, a3, a4};
    *(v4u*)(hc + 3 * HC_STRIDE) = (v4u){P[j][2], P[j][3], Q[j][0], Q[j][1]};
    if (j == 0 && t == 0) {
      char* h0 = lds + L_HC + ch * 4 * HC_STRIDE;
      const v4u z = {0, 0, 0, 0};
#pragma unroll
      for (int c = 0; c < 4; ++c) *(v4u*)(h0 + c * HC_STRIDE) = z;
      *(v4u*)(h0 + 16 + 0 * HC_STRIDE) = (v4u){0, 0, 0, P[0][0] << 16};
      *(v4u*)(h0 + 16 + 1 * HC_STRIDE) = (v4u){0, 0, 0, P[0][0]};
      *(v4u*)(h0 + 16 + 2 * HC_STRIDE) = (v4u){0, 0, P[0][0] << 16, a0};
      *(v4u*)(h0 + 16 + 3 * HC_STRIDE) = (v4u){0, 0, P[0][0], P[0][1]};
    }
  }
  if (t < 32) *(v4u*)(lds + L_U + ch * U_STRIDE + 4096 + t * 16) = (v4u){0, 0, 0, 0};
  bar();
  bar();
  char* tb = lds + L_TILE + wq * 6144;
#pragma unroll
  for (int j = 0; j < NUN; ++j) {
#pragma unroll
    for (int hh = 0; hh < 2; ++hh) *(v4u*)(tb + j * 2048 + hh * 1024 + lane * 16) = tl[j][hh];
    const LAS char* ra = (const LAS char*)tb + j * 2048 + (8 * (lane >> 5) + ((lane & 15) >> 2)) * 128 + ((lane >> 4) & 1) * 32 + (lane & 3) * 8;
    float cs[2];
#pragma unroll
    for (int n2 = 0; n2 < 2; ++n2) {
      const v2u lo = __builtin_bit_cast(v2u, __builtin_amdgcn_ds_read_tr16_b64_v4i16((LAS v4s*)(ra + n2 * 64)));
      const v2u hi = __builtin_bit_cast(v2u, __builtin_amdgcn_ds_read_tr16_b64_v4i16((LAS v4s*)(ra + n2 * 64 + 512)));
      cstore((v4u){lo[0], lo[1], hi[0], hi[1]}, rws, tdst[j] + n2 * tstr[j] + lane * 16);
      cs[n2] = dot2(lo[0], 0x3f803f80u, dot2(lo[1], 0x3f803f80u, dot2(hi[0], 0x3f803f80u, dot2(hi[1], 0x3f803f80u, 0.f))));
    }
    const float cst = tr32(cs[0], cs[1]);
    cstore1(cst, rws, tcs[j] >= 0 ? OFF_CSP + (u32)(tcs[j] + lane) * 4 : 0x80000000u);
  }
  if (MOD) {
    float acc[8] = {};
#pragma unroll
    for (int j = 0; j < 8; ++j) {
      const float c = lo2f(cc[j]); float s = c * __builtin_amdgcn_rcpf(1.f + __builtin_amdgcn_exp2f(-1.442695041f * c)); s = lo2f(pk2(s, s));
#pragma unroll
      for (int e = 0; e < 4; ++e) { acc[2 * e] += s * lo2f(mx[j][e]); acc[2 * e + 1] += s * hi2f(mx[j][e]); }
    }
    const float b0 = tr32(acc[0], acc[4]), b1 = tr32(acc[1], acc[5]), b2 = tr32(acc[2], acc[6]), b3 = tr32(acc[3], acc[7]);
    float e0 = tr16(b0, b2), e1 = tr16(b1, b3);
    e0 += dpp<0x128>(e0); e1 += dpp<0x128>(e1);
    if (!(lane & 8)) *(v2f*)(lds + L_MODRED + wq * 256 + (8 * (lane & 7) + 2 * (lane >> 4)) * 4) = (v2f){e0, e1};
  }
#pragma unroll
  for (int j = 0; j < 8; ++j) *(v4u*)(lds + L_Q + j * 4096 + t2 * 16) = qs[j];
  DRAIN();
  u32 arr = 0;
  if (lane == 0) arr = __hip_atomic_fetch_add((LAS u32*)(lds + L_CNT), 1u, __ATOMIC_ACQ_REL, __HIP_MEMORY_SCOPE_WORKGROUP);
  if (__builtin_amdgcn_readfirstlane(arr) == 3) {
    if (MOD) {
      const float* mr = (const float*)(lds + L_MODRED);
      cstore1(mr[lane] + mr[64 + lane] + mr[128 + lane] + mr[192 + lane], rws, OFF_MODP + (u32)((b & 1) * 3072 + 64 * (b >> 1) + lane) * 4);
      DRAIN();
    }
    if (lane == 0) post(a.ws, C_FW + b * 8, tag);
  }
}

__global__ __launch_bounds__(512, 2) void mega(Args a) {
  __shared__ __attribute__((aligned(16))) char lds[LDS_BYTES];
  const int b = blockIdx.x, tid = threadIdx.x, lane = tid & 63, w = __builtin_amdgcn_readfirstlane(tid >> 6);
  const int g = b & 7, bi = b >> 3;
  const rsrc_t rws = mk_rsrc(a.ws, WS_BYTES);
  STAMP(0); CSTAMP(28);
  u64 tag = *(const __attribute__((address_space(4))) u64*)(a.ws + C_E);
  const int d0 = 64 * g + 2 * bi;
  const int tt = 8 * g + (bi >> 2), cgp = bi & 3;
  const int rg = 4 * g + (bi >> 3), cj = bi & 7;
  u32 qv[4][4];

  if (w < 4) {
    const int cg = tid & 7, rp = tid >> 3;
    const int r = 64 * bi + 2 * rp, c0 = 64 * g + 8 * cg;
    const rsrc_t rk = mk_rsrc(a.k, S * D * 2), rv = mk_rsrc(a.v, S * D * 2);
    v4u kk[5], vv[2], sc[4];
#pragma unroll
    for (int j = 0; j < 5; ++j) { const int rr = r - 3 + j; kk[j] = bload(rk, rr >= 0 ? (u32)((rr * D + c0) * 2) : 0x80000000u, 0); }
#pragma unroll
    for (int j = 0; j < 2; ++j) vv[j] = bload(rv, (u32)(((r + j) * D + c0) * 2), 0);
#pragma unroll
    for (int j = 0; j < 4; ++j) sc[j] = *(const v4u*)(a.sh + c0 * 4 + 8 * j);
    SB();
    asm volatile("" : "+s"(tag));
    tag += 1;
    if (tid == 0) *(u32*)(lds + L_CNT) = 0;
    const int pos = 31 - rp;
    char* tb = lds + L_T + cg * 8 * 128 + ((((pos >> 2) ^ cg) << 2) + (pos & 3)) * 4;
#pragma unroll
    for (int c = 0; c < 8; ++c) {
      float s0 = lo2f(sc[c >> 1][(c & 1) * 2]), s1 = hi2f(sc[c >> 1][(c & 1) * 2]), s2 = lo2f(sc[c >> 1][(c & 1) * 2 + 1]), s3 = hi2f(sc[c >> 1][(c & 1) * 2 + 1]);
      float kf[5], vf[2];
#pragma unroll
      for (int j = 0; j < 5; ++j) kf[j] = (c & 1) ? hi2f(kk[j][c >> 1]) : lo2f(kk[j][c >> 1]);
#pragma unroll
      for (int j = 0; j < 2; ++j) vf[j] = (c & 1) ? hi2f(vv[j][c >> 1]) : lo2f(vv[j][c >> 1]);
      float a0 = kf[0] * s3 + kf[1] * s2 + kf[2] * s1 + kf[3] * s0;
      float a1 = kf[1] * s3 + kf[2] * s2 + kf[3] * s1 + kf[4] * s0;
      *(u32*)(tb + c * 128) = pk2(a1 * vf[1], a0 * vf[0]);
    }
    bar();
    STAMP(1);
#pragma unroll
    for (int j = 0; j < 2; ++j) {
      const int ch = (tid >> 3) + 32 * j, ck = tid & 7;
      const v4u o = *(const v4u*)(lds + L_T + ch * 128 + ((ck ^ (ch >> 3)) * 16));
      cstore(o, rws, OFF_KVT + (u32)(64 * g + ch) * 4096 + (u32)(31 - bi) * 128 + ck * 16);
    }
    if (bi == 0 && tid < 8) post(a.ws, C_CB + (tid * 8 + g) * 128, tag << 8);
    DRAIN();
    bar();
    if (tid == 0) post(a.ws, C_FA + g * 256 + bi * 8, tag);
    STAMP(2);
  } else if (b < 96) prep<true>(a, lds, rws, b, tid, lane, w, d0, tt, tag);
  else prep<false>(a, lds, rws, b, tid, lane, w, d0, tt, tag);
  const u32 dd = *(const u32*)(a.sD + d0);
  SB();

  {
    const int ch = w & 1, pset = (w >> 1) & 1, lh = w >> 2;
    const int m = lane & 15, kg = lane >> 4;
    const LAS char* hbase = (const LAS char*)lds + L_HC + ch * 4 * HC_STRIDE + (m & 3) * HC_STRIDE + 2 * (8 * kg + (m & 12)) + lh * 4 * 64;
    const LAS char* ubase = (const LAS char*)lds + L_U + ch * U_STRIDE + 2 * (2032 - 16 * m + 8 * kg) + lh * 4 * 64 - 7 * 512;
    v4u A[4][8];
#pragma unroll
    for (int lp = 0; lp < 4; ++lp)
#pragma unroll
      for (int j = 0; j < 8; ++j) {
        const v2u x0 = *(const volatile LAS v2u*)(hbase + lp * 64 + j * 512), x1 = *(const volatile LAS v2u*)(hbase + lp * 64 + j * 512 + 8);
        A[lp][j] = (v4u){x0[0], x0[1], x1[0], x1[1]};
      }
    SB();
    STAMP(3);
    if (w == 0) {
      poll(a.ws, C_FA + g * 256 + (lane & 31) * 8, true, tag);
      v4u uu[8];
#pragma unroll
      for (int j = 0; j < 8; ++j) uu[j] = cload(rws, lane * 16, OFF_KVT + (u32)d0 * 4096 + j * 1024);
      SB();
#pragma unroll
      for (int j = 0; j < 8; ++j) *(v4u*)(lds + L_U + (j >> 2) * U_STRIDE + (j & 3) * 1024 + lane * 16) = uu[j];
    }
    bar();
    STAMP(4);
    v4f acc[4] = {};
    v4u B[2][8];
#pragma unroll
    for (int j = 0; j < 8; ++j) B[0][j] = *(const LAS v4u*)(ubase + (7 - j) * 512);
    SB();
#define CONV_STEPS(P0, P1, P2, P3) \
    _Pragma("unroll") for (int lp = 0; lp < 4; ++lp) { \
      if (lp < 3) { _Pragma("unroll") for (int j = 0; j < 8; ++j) B[(lp + 1) & 1][j] = *(const LAS v4u*)(ubase + (lp + 1) * 64 + (7 - j) * 512); } \
      SB(); \
      _Pragma("unroll") for (int qq = 0; qq <= P3; ++qq) { \
        if (qq <= P0) acc[0] = mfma16(A[lp][P0 - qq], B[lp & 1][qq], acc[0]); \
        if (qq <= P1) acc[1] = mfma16(A[lp][P1 - qq], B[lp & 1][qq], acc[1]); \
        if (qq <= P2) acc[2] = mfma16(A[lp][P2 - qq], B[lp & 1][qq], acc[2]); \
        acc[3] = mfma16(A[lp][P3 - qq], B[lp & 1][qq], acc[3]); \
      } \
      PIN(acc[0]); PIN(acc[1]); PIN(acc[2]); PIN(acc[3]); \
      SB(); \
    }
    if (pset == 0) { CONV_STEPS(0, 3, 4, 7) } else { CONV_STEPS(1, 2, 5, 6) }
    STAMP(5);
    {
      char* red = lds + L_CRED + (lh * 2 + ch) * 8192;
      const int Ql = 4 * m + kg, Qs = Ql ^ ((Ql >> 3) & 3);
#pragma unroll
      for (int i = 0; i < 4; ++i) {
        const int p = pset == 0 ? (i == 0 ? 0 : i == 1 ? 3 : i == 2 ? 4 : 7) : (i == 0 ? 1 : i == 1 ? 2 : i == 2 ? 5 : 6);
        *(v4f*)(red + p * 1024 + Qs * 16) = acc[i];
      }
    }
    if (w == 7) {
#pragma unroll 1
      for (int j = 0; j < 4; ++j) poll(a.ws, C_FW + (64 * j + lane) * 8, true, tag);
    }
    bar();
    {
      const char* red = lds + L_CRED + (tid ^ ((tid >> 3) & 3)) * 16;
      const v4f y0 = *(const v4f*)red + *(const v4f*)(red + 16384), y1 = *(const v4f*)(red + 8192) + *(const v4f*)(red + 24576);
      const v2u u0 = *(const v2u*)(lds + L_U + (2044 - 4 * tid) * 2), u1 = *(const v2u*)(lds + L_U + U_STRIDE + (2044 - 4 * tid) * 2);
      const float D0 = lo2f(dd), D1 = hi2f(dd);
      const v4u o = {pk2(y0[0] + D0 * hi2f(u0[1]), y1[0] + D1 * hi2f(u1[1])), pk2(y0[1] + D0 * lo2f(u0[1]), y1[1] + D1 * lo2f(u1[1])),
                     pk2(y0[2] + D0 * hi2f(u0[0]), y1[2] + D1 * hi2f(u1[0])), pk2(y0[3] + D0 * lo2f(u0[0]), y1[3] + D1 * lo2f(u1[0]))};
      cstore(o, rws, OFF_Z + (u32)(tid >> 3) * 32768 + (u32)(d0 >> 1) * 128 + (tid & 7) * 16);
    }
    DRAIN();
    bar();
    if (tid < 8) __hip_atomic_fetch_add((GAS u64*)(a.ws + C_CB + (tid * 8 + g) * 128), 1ull, RLX_AGENT);
    STAMP(6);
  }

  v4u wA[16], wB[16];
  {
    float mp[12];
#pragma unroll
    for (int i = 0; i < 12; ++i) mp[i] = cload1(rws, tid * 4, OFF_MODP + (u32)((i & 1) * 3072 + (i >> 1) * D) * 4);
    SB();
#pragma unroll
    for (int s = 0; s < 16; ++s) wA[s] = cload(rws, lane * 16, OFF_W1 + (u32)((4 * cgp + (w & 3)) * 32 + (w >> 2) * 16 + s) * 1024);
    SB();
    float md[6];
#pragma unroll
    for (int i = 0; i < 6; ++i) { const float t = mp[2 * i] + mp[2 * i + 1]; md[i] = lo2f(pk2(t, t)); }
    float* V = (float*)(lds + L_V) + tid;
    V[0] = md[2] * (1.f + md[1]); V[D] = md[2] * md[0]; V[2 * D] = 1.f + md[4]; V[3 * D] = md[3]; V[4 * D] = md[5];
  }
  float x[4][4][2];
  v2f Cv[4], Ev[4];
  {
    float st[8] = {}, rs[4], mo[4];
#pragma unroll
    for (int ci = 0; ci < 4; ++ci)
#pragma unroll
      for (int t = 0; t < 4; ++t) qv[ci][t] = *(const u32*)(lds + L_Q + (4 * w + t) * 1024 + ((cgp + ci) & 3) * 256 + lane * 4);
#pragma unroll
    for (int ci = 0; ci < 4; ++ci)
#pragma unroll
      for (int t = 0; t < 4; ++t) { st[2 * t] = dot2(qv[ci][t], 0x3f803f80u, st[2 * t]); st[2 * t + 1] = dot2(qv[ci][t], qv[ci][t], st[2 * t + 1]); }
    ln_stats(st, lane, rs, mo);
    bar();
#pragma unroll
    for (int ci = 0; ci < 4; ++ci) {
      const float* V = (const float*)(lds + L_V) + 128 * ((cgp + ci) & 3) + 2 * lane;
      const v2f Av = *(const v2f*)V, Bv = *(const v2f*)(V + D);
      Cv[ci] = *(const v2f*)(V + 2 * D); Ev[ci] = *(const v2f*)(V + 3 * D);
#pragma unroll
      for (int t = 0; t < 4; ++t) {
        x[ci][t][0] = (lo2f(qv[ci][t]) * rs[t] + mo[t]) * Av[0] + Bv[0];
        x[ci][t][1] = (hi2f(qv[ci][t]) * rs[t] + mo[t]) * Av[1] + Bv[1];
        PIN(x[ci][t][0]); PIN(x[ci][t][1]);
      }
    }
  }
  STAMP(7);
  if (w == 7) poll(a.ws, C_CB + (g * 8 + (lane & 7)) * 128, true, (tag << 8) + 32);
  bar();
  STAMP(8);
  if (b == 0 && tid == 0) post(a.ws, C_E, tag);
  {
    v4u zs[4];
#pragma unroll
    for (int i = 0; i < 4; ++i) zs[i] = cload(rws, tid * 16, OFF_Z + (u32)tt * 32768 + i * 8192);
    SB();
#pragma unroll
    for (int s = 0; s < 16; ++s) wB[s] = cload(rws, lane * 16, OFF_W2 + (u32)((4 * cgp + (w & 3)) * 32 + (w >> 2) * 16 + s) * 1024);
    SB();
#pragma unroll
    for (int i = 0; i < 4; ++i) *(v4u*)(lds + L_ZS + i * 8192 + (tid ^ ((tid >> 4) & 7)) * 16) = zs[i];
    bar();
    STAMP(9);
    float st[8] = {}, rs[4], mo[4];
#pragma unroll
    for (int ci = 0; ci < 4; ++ci) {
      const v4u z = *(const v4u*)(lds + L_ZS + ((cgp + ci) & 3) * 8192 + lane * 128 + ((w ^ ((lane >> 1) & 7)) * 16));
#pragma unroll
      for (int t = 0; t < 4; ++t) {
        const float x0 = x[ci][t][0] * lo2f(z[t]) + lo2f(qv[ci][t]), x1 = x[ci][t][1] * hi2f(z[t]) + hi2f(qv[ci][t]);
        x[ci][t][0] = x0; x[ci][t][1] = x1;
        st[2 * t] += x0 + x1; st[2 * t + 1] += x0 * x0 + x1 * x1;
      }
    }
#pragma unroll
    for (int t = 0; t < 4; ++t) *(u32*)(lds + L_XH + (4 * w + t) * 264 + lane * 4) = pk2(x[0][t][0], x[0][t][1]);
    ln_stats(st, lane, rs, mo);
#pragma unroll
    for (int i = 0; i < 8; ++i) st[i] = 0;
#pragma unroll
    for (int ci = 0; ci < 4; ++ci)
#pragma unroll
      for (int t = 0; t < 4; ++t) {
        const float h0 = (x[ci][t][0] * rs[t] + mo[t]) * Cv[ci][0] + Ev[ci][0], h1 = (x[ci][t][1] * rs[t] + mo[t]) * Cv[ci][1] + Ev[ci][1];
        x[ci][t][0] = h0; x[ci][t][1] = h1;
        st[2 * t] += h0 + h1; st[2 * t + 1] += h0 * h0 + h1 * h1;
      }
#pragma unroll
    for (int t = 0; t < 4; ++t) *(u32*)(lds + L_XH + 32 * 264 + (4 * w + t) * 264 + lane * 4) = pk2(x[0][t][0], x[0][t][1]);
    ln_stats(st, lane, rs, mo);
#pragma unroll
    for (int t = 0; t < 4; ++t) {
      v4u o;
#pragma unroll
      for (int ci = 0; ci < 4; ++ci) o[ci] = pk2(x[ci][t][0] * rs[t] + mo[t], x[ci][t][1] * rs[t] + mo[t]);
      *(v4u*)(lds + L_X + (lane >> 1) * 1024 + ((((lane & 1) * 32 + 4 * w + t) ^ (lane & 7)) * 16)) = o;
    }
    bar();
    STAMP(10);
  }
  const u32 fro = (u32)tt * 32768 + (u32)(2 * (4 * cgp + (w & 3)) + (w >> 2)) * 1024 + lane * 16;
  {
    v4f m0, m1;
    gemm_small<true>(wA, lds, w, lane, m0, m1);
    cstore((v4u){pk2(gelu_t(m0[0]), gelu_t(m0[1])), pk2(gelu_t(m0[2]), gelu_t(m0[3])), pk2(gelu_t(m1[0]), gelu_t(m1[1])), pk2(gelu_t(m1[2]), gelu_t(m1[3]))}, rws, OFF_G + fro);
    DRAIN();
    bar();
    if (tid == 448) post(a.ws, C_FG + (tt * 4 + cgp) * 8, tag);
    STAMP(11);
  }
  v4u w3[32];
  const u32 w3o = OFF_W3 + (u32)(7 * cj + (w < 7 ? w : 6)) * 32768;
  if (w < 7) {
#pragma unroll
    for (int s = 0; s < 16; ++s) w3[s] = cload(rws, lane * 16, w3o + s * 1024);
  } else {
    const int ft = 4 * (tt - 1) + lane;
    poll(a.ws, C_FG + ft * 8, lane < 12 && ft >= 0 && ft < 256, tag);
  }
  SB();
  bar();
  STAMP(12);
  {
    const int n = lane & 31, h = lane >> 5, sg = w >> 1;
    const int t = 32 * tt + n + (sg == 0 ? -1 : sg == 1 ? 1 : sg == 2 ? -2 : 2);
    const u32 off = (t >= 0 && t < S) ? (u32)((t >> 5) * 32768 + (h * 32 + (t & 31)) * 16) : 0x80000000u;
    v4u xs[4];
#pragma unroll
    for (int i = 0; i < 4; ++i) xs[i] = cload(rws, off, OFF_G + (u32)(4 * w + i) * 1024);
    SB();
#pragma unroll
    for (int i = 0; i < 4; ++i) *(v4u*)(lds + L_X + (4 * w + i) * 1024 + lane * 16) = xs[i];
    bar();
    STAMP(13);
    v4f m0, m1;
    gemm_small<false>(wB, lds, w, lane, m0, m1);
    const char* xh = lds + L_XH + n * 264 + (32 * (w & 3) + 16 * (w >> 2) + 4 * h) * 2;
    const v2u xa = *(const v2u*)xh, xb = *(const v2u*)(xh + 16), ha = *(const v2u*)(xh + 32 * 264), hb = *(const v2u*)(xh + 32 * 264 + 16);
    cstore((v4u){pk2(m0[0] + lo2f(ha[0]), m0[1] + hi2f(ha[0])), pk2(m0[2] + lo2f(ha[1]), m0[3] + hi2f(ha[1])),
                 pk2(m1[0] + lo2f(hb[0]), m1[1] + hi2f(hb[0])), pk2(m1[2] + lo2f(hb[1]), m1[3] + hi2f(hb[1]))}, rws, OFF_X1P + fro);
    cstore((v4u){xa[0], xa[1], xb[0], xb[1]}, rws, OFF_X1 + fro);
    DRAIN();
    bar();
    if (tid == 448) post(a.ws, C_FX + (tt * 4 + cgp) * 8, tag);
    STAMP(14);
  }
  v4u w4[2][14];
  const u32 w4o = OFF_W4 + (u32)(2 * cj * 112 + 14 * w) * 1024;
  const u32 fo4 = (u32)(w >> 2) * 32768 + (u32)(4 * cj + (w & 3)) * 1024 + lane * 16;
  v4u x1f, xpf;
  if (w < 7) {
#pragma unroll
    for (int s = 16; s < 32; ++s) w3[s] = cload(rws, lane * 16, w3o + s * 1024);
  } else {
    poll(a.ws, C_FX + (rg * 8 + (lane & 7)) * 8, true, tag);
  }
  SB();
  bar();
  STAMP(15);
  {
    const int n = lane & 31, h = lane >> 5;
    v4u xs[8];
#pragma unroll
    for (int i = 0; i < 8; ++i) xs[i] = cload(rws, lane * 16, OFF_X1P + (u32)rg * 65536 + (u32)(8 * w + i) * 1024);
    x1f = cload(rws, fo4, OFF_X1 + (u32)rg * 65536);
    SB();
#pragma unroll
    for (int i = 0; i < 8; ++i) *(v4u*)(lds + L_X + (8 * w + i) * 1024 + lane * 16) = xs[i];
    float s1 = 0, s2 = 0;
#pragma unroll
    for (int i = 0; i < 8; ++i)
#pragma unroll
      for (int e = 0; e < 4; ++e) { s1 = dot2(xs[i][e], 0x3f803f80u, s1); s2 = dot2(xs[i][e], xs[i][e], s2); }
    *(float*)(lds + L_ST + w * 256 + n * 8 + h * 4) = tr32(s1, s2);
    bar();
    STAMP(16); CSTAMP(30);
    xpf = *(const v4u*)(lds + L_X + fo4);
    if (w == 7) {
      const int cl = lane < 56 ? lane : 55;
      v4u cp[32];
#pragma unroll
      for (int j = 0; j < 32; ++j) cp[j] = cload(rws, cl * 16, OFF_CSP + (u32)(j * F + 224 * cj) * 4);
      SB();
      v4f cs = {0, 0, 0, 0};
#pragma unroll
      for (int j = 0; j < 32; ++j) cs += __builtin_bit_cast(v4f, cp[j]);
      *(v4f*)(lds + L_CS + cl * 16) = cs;
      SB();
#pragma unroll
      for (int s = 0; s < 14; ++s)
#pragma unroll
        for (int i = 0; i < 2; ++i) w4[i][s] = cload(rws, lane * 16, w4o + (u32)(i * 112 + s) * 1024);
      SB();
      bar();
    } else {
      v16f acc[2] = {};
      v4u xa[2][2][2];
#pragma unroll
      for (int i = 0; i < 2; ++i)
#pragma unroll
        for (int e = 0; e < 2; ++e) xa[0][i][e] = *(const v4u*)(lds + L_X + i * 32768 + e * 1024 + lane * 16);
#pragma unroll
      for (int s2 = 0; s2 < 16; ++s2) {
        if (s2 < 15) {
#pragma unroll
          for (int i = 0; i < 2; ++i)
#pragma unroll
            for (int e = 0; e < 2; ++e) xa[(s2 + 1) & 1][i][e] = *(const v4u*)(lds + L_X + i * 32768 + (2 * s2 + 2 + e) * 1024 + lane * 16);
        }
        SB();
#pragma unroll
        for (int e = 0; e < 2; ++e)
#pragma unroll
          for (int i = 0; i < 2; ++i) acc[i] = mfma32(w3[2 * s2 + e], xa[s2 & 1][i][e], acc[i]);
        PIN(acc[0]); PIN(acc[1]);
        SB();
        if (s2 < 14) {
#pragma unroll
          for (int i = 0; i < 2; ++i) w4[i][s2] = cload(rws, lane * 16, w4o + (u32)(i * 112 + s2) * 1024);
        }
      }
      bar();
      STAMP(17); CSTAMP(31);
      v4f cs[4];
#pragma unroll
      for (int q = 0; q < 4; ++q) cs[q] = *(const v4f*)(lds + L_CS + (32 * w + 8 * q + 4 * h) * 4);
#pragma unroll
      for (int i = 0; i < 2; ++i) {
        const char* sp = lds + L_ST + i * 1024 + n * 8;
        v2f st = *(const v2f*)sp + *(const v2f*)(sp + 256) + *(const v2f*)(sp + 512) + *(const v2f*)(sp + 768);
        st *= (1.f / D);
        const float rs = __builtin_amdgcn_rsqf(fmaxf(st[1] - st[0] * st[0], 0.f) + 1e-5f), mo = -st[0] * rs;
#pragma unroll
        for (int s = 0; s < 2; ++s) {
          v4u o;
#pragma unroll
          for (int jj = 0; jj < 4; ++jj) {
            const int r0 = 8 * s + 2 * jj;
            o[jj] = pk2(gelu_t(acc[i][r0] * rs + cs[r0 >> 2][r0 & 3] * mo), gelu_t(acc[i][r0 + 1] * rs + cs[r0 >> 2][(r0 & 3) + 1] * mo));
          }
          cstore(o, rws, OFF_H + (u32)(2 * rg + i) * (112 * 1024) + (u32)(2 * (7 * cj + w) + s) * 1024 + lane * 16);
        }
      }
    }
    DRAIN();
    bar();
    if (tid == 448) post(a.ws, C_FH + (rg * 8 + cj) * 8, tag);
    STAMP(18);
  }
  if (w == 7) poll(a.ws, C_FH + (rg * 8 + (lane & 7)) * 8, true, tag);
  bar();
  STAMP(19);
  {
    const int n = lane & 31, h = lane >> 5;
    v4u xf[14];
    const u32 xo = OFF_H + (u32)(2 * rg) * (112 * 1024) + (u32)(14 * w) * 1024;
#pragma unroll
    for (int s = 0; s < 14; ++s) xf[s] = cload(rws, lane * 16, xo + s * 1024);
    SB();
#pragma unroll
    for (int i = 0; i < 2; ++i) {
      v16f acc[2] = {};
#pragma unroll
      for (int s = 0; s < 14; ++s) {
        acc[0] = mfma32(w4[0][s], xf[s], acc[0]); acc[1] = mfma32(w4[1][s], xf[s], acc[1]);
        PIN(acc[0]); PIN(acc[1]);
        SB();
        if (i == 0) { xf[s] = cload(rws, lane * 16, xo + 112 * 1024 + s * 1024); SB(); }
      }
#pragma unroll
      for (int j = 0; j < 2; ++j)
#pragma unroll
        for (int q = 0; q < 4; ++q)
          *(v4f*)(lds + L_RED4 + (4 * i + 2 * j + (q >> 1)) * 16384 + w * 2048 + (q & 1) * 1024 + lane * 16) = (v4f){acc[j][4 * q], acc[j][4 * q + 1], acc[j][4 * q + 2], acc[j][4 * q + 3]};
    }
    bar();
    STAMP(20);
    v4f m0 = {0, 0, 0, 0}, m1 = {0, 0, 0, 0};
#pragma unroll
    for (int sw = 0; sw < 8; ++sw) { m0 += *(const v4f*)(lds + L_RED4 + w * 16384 + sw * 2048 + lane * 16); m1 += *(const v4f*)(lds + L_RED4 + w * 16384 + sw * 2048 + 1024 + lane * 16); }
    const int f0 = 64 * cj + 16 * (w & 3);
    const float* gb = (const float*)(lds + L_V) + 4 * D + f0 + 4 * h;
    const v4f g0 = *(const v4f*)gb, g1 = *(const v4f*)(gb + 8);
    const u32 o0 = pk2(lo2f(x1f[0]) + g0[0] * (lo2f(xpf[0]) + m0[0]), hi2f(x1f[0]) + g0[1] * (hi2f(xpf[0]) + m0[1]));
    const u32 o1 = pk2(lo2f(x1f[1]) + g0[2] * (lo2f(xpf[1]) + m0[2]), hi2f(x1f[1]) + g0[3] * (hi2f(xpf[1]) + m0[3]));
    const u32 o2 = pk2(lo2f(x1f[2]) + g1[0] * (lo2f(xpf[2]) + m1[0]), hi2f(x1f[2]) + g1[1] * (hi2f(xpf[2]) + m1[1]));
    const u32 o3 = pk2(lo2f(x1f[3]) + g1[2] * (lo2f(xpf[3]) + m1[2]), hi2f(x1f[3]) + g1[3] * (hi2f(xpf[3]) + m1[3]));
    auto e0 = __builtin_amdgcn_permlane32_swap(o0, o2, false, false);
    auto e1 = __builtin_amdgcn_permlane32_swap(o1, o3, false, false);
    *(v4u*)(a.out + (size_t)(64 * rg + 32 * (w >> 2) + n) * D + f0 + 8 * h) = (v4u){e0[0], e1[0], e0[1], e1[1]};
  }
}

extern "C" void kernel_launch(void* const* d_in, const int* in_sizes, int n_in, void* d_out, int out_size, void* d_ws, size_t ws_size, hipStream_t stream) {
  Args a;
  a.q = (const u16*)d_in[0]; a.k = (const u16*)d_in[1]; a.v = (const u16*)d_in[2]; a.sh = (const u16*)d_in[3]; a.sk = (const u16*)d_in[4]; a.sD = (const u16*)d_in[5];
  a.w1 = (const u16*)d_in[7]; a.w2 = (const u16*)d_in[8]; a.w3 = (const u16*)d_in[9]; a.w4 = (const u16*)d_in[10]; a.c = (const u16*)d_in[11]; a.wm = (const u16*)d_in[12];
  a.out = (u16*)d_out; a.ws = (char*)d_ws;
  mega<<<256, 512, 0, stream>>>(a);
}
```

```cpp
#include <hip/hip_runtime.h>
#include <stdint.h>

typedef unsigned short u16;
typedef unsigned int u32;
typedef unsigned long long u64;
typedef u32 v4u __attribute__((ext_vector_type(4)));
typedef u32 v2u __attribute__((ext_vector_type(2)));
typedef short v4s __attribute__((ext_vector_type(4)));
typedef float v2f __attribute__((ext_vector_type(2)));
typedef float v4f __attribute__((ext_vector_type(4)));
typedef __bf16 bf8 __attribute__((ext_vector_type(8)));
typedef __bf16 bf2 __attribute__((ext_vector_type(2)));
#define DI __device__ __forceinline__
#define LAS __attribute__((address_space(3)))
#define GAS __attribute__((address_space(1)))
#define SB() __builtin_amdgcn_sched_barrier(0)
#define PIN(x) asm volatile("" : "+v"(x))
#define DRAIN() asm volatile("s_waitcnt vmcnt(0)" ::: "memory")
#define VMWAIT(n) asm volatile("s_waitcnt vmcnt(" #n ")" ::: "memory")
#define RLX_AGENT __ATOMIC_RELAXED, __HIP_MEMORY_SCOPE_AGENT

constexpr int S = 2048, D = 512, F = 1792;
constexpr u32 MB = 1u << 20;
constexpr u32 FL = 128;
constexpr u32 C_E = 0, C_FW = 4096, C_FW2 = 6144, C_CNT = 8192, C_FB = 12288;
constexpr u32 C_FA = 65536, C_FG = C_FA + 256 * FL, C_FX = C_FG + 256 * FL, C_FH = C_FX + 256 * FL;
constexpr u32 OFF_MODP = 256 * 1024;
constexpr u32 OFF_CSP = 320 * 1024;
constexpr u32 CSW = 2304;
constexpr u32 OFF_KVT = 1 * MB;
constexpr u32 OFF_Z = 3 * MB;
constexpr u32 OFF_G = 5 * MB;
constexpr u32 OFF_X1 = 7 * MB;
constexpr u32 OFF_X1P = 9 * MB;
constexpr u32 OFF_H = 11 * MB;
constexpr u32 OFF_W1 = 18 * MB;
constexpr u32 OFF_W2 = OFF_W1 + 512 * 1024;
constexpr u32 OFF_W3 = 19 * MB;
constexpr u32 OFF_W4 = 21 * MB;
constexpr u32 WS_BYTES = 24 * MB;
constexpr u32 OOB = 0x80000000u;

constexpr int HC_STRIDE = 4160;
constexpr int L_HC = 0;
constexpr int L_U = 33280;
constexpr int U_STRIDE = 4608;
constexpr int L_T = 43008;
constexpr int L_TILE = 51200;
constexpr int L_CRED = 75776;
constexpr int L_Q = 108544;
constexpr int L_X = 0;
constexpr int L_ZS = 67584;
constexpr int L_RED4 = 0;
constexpr int L_XH = 131072;
constexpr int L_V = 147968;
constexpr int L_MISC = 158208;
constexpr int L_MODRED = L_MISC;
constexpr int L_CS3 = L_MISC;
constexpr int L_ST = L_MISC + 1024;
constexpr int L_ST1 = L_MISC + 3072;
constexpr int L_CNT = L_MISC + 3328;
constexpr int LDS_BYTES = L_CNT + 32;

#define STAMP(slot) do {} while (0)
#define STAMP_INIT() do {} while (0)
#define CSTAMP(slot) do {} while (0)

DI float lo2f(u32 x) { return __uint_as_float(x << 16); }
DI float hi2f(u32 x) { return __uint_as_float(x & 0xffff0000u); }
DI u32 pk2(float lo, float hi) { v2f t = {lo, hi}; bf2 b = __builtin_convertvector(t, bf2); return __builtin_bit_cast(u32, b); }
DI v4f mfma16(v4u a, v4u b, v4f c) { return __builtin_amdgcn_mfma_f32_16x16x32_bf16(__builtin_bit_cast(bf8, a), __builtin_bit_cast(bf8, b), c, 0, 0, 0); }
template <int CTRL> DI float dpp(float x) { return __builtin_bit_cast(float, __builtin_amdgcn_mov_dpp(__builtin_bit_cast(int, x), CTRL, 0xf, 0xf, true)); }
DI float red16(float x) { x += dpp<0xB1>(x); x += dpp<0x4E>(x); x += dpp<0x141>(x); x += dpp<0x140>(x); return x; }
DI float tr32(float a, float b) { auto r = __builtin_amdgcn_permlane32_swap(__float_as_uint(a), __float_as_uint(b), false, false); return __uint_as_float(r[0]) + __uint_as_float(r[1]); }
DI float tr16(float a, float b) { auto r = __builtin_amdgcn_permlane16_swap(__float_as_uint(a), __float_as_uint(b), false, false); return __uint_as_float(r[0]) + __uint_as_float(r[1]); }
DI float dot2(u32 a, u32 b, float c) { return __builtin_amdgcn_fdot2_f32_bf16(__builtin_bit_cast(bf2, a), __builtin_bit_cast(bf2, b), c, false); }
DI float rdl(float x, int l) { return __builtin_bit_cast(float, __builtin_amdgcn_readlane(__builtin_bit_cast(int, x), l)); }
struct Gelu { float k1, k2, irs; };
DI Gelu gelu_coef(float var) {
  const float v = fmaxf(var, 0.f) + 1e-5f, rs = __builtin_amdgcn_rsqf(v);
  return Gelu{-2.302208198f * rs, -0.1029432397f * rs * rs * rs, v * rs};
}
DI float gelu_s(float a, const Gelu& c) {
  const float e = __builtin_amdgcn_exp2f(a * __builtin_fmaf(a * a, c.k2, c.k1));
  float y = a * __builtin_amdgcn_rcpf(__builtin_fmaf(e, c.irs, c.irs));
  asm("" : "+v"(y));
  return y;
}
DI v2f pfma(v2f a, v2f b, v2f c) { return __builtin_elementwise_fma(a, b, c); }
DI v2f bc(float x) { return (v2f){x, x}; }
DI u32 pkv(v2f t) { bf2 b = __builtin_convertvector(t, bf2); return __builtin_bit_cast(u32, b); }
constexpr u32 ONES = 0x3f803f80u;
typedef __amdgpu_buffer_rsrc_t rsrc_t;
DI rsrc_t mk_rsrc(const void* p, u32 bytes) { return __builtin_amdgcn_make_buffer_rsrc((void*)p, 0, bytes, 0x00020000); }
DI v4u bload(rsrc_t r, u32 voff, u32 soff) { return __builtin_amdgcn_raw_buffer_load_b128(r, voff, soff, 0); }
DI v4u cload(rsrc_t r, u32 voff, u32 soff) { return __builtin_amdgcn_raw_buffer_load_b128(r, voff, soff, 16); }
DI v2u cload2(rsrc_t r, u32 voff, u32 soff) { return __builtin_amdgcn_raw_buffer_load_b64(r, voff, soff, 16); }
DI float cload1(rsrc_t r, u32 voff, u32 soff) { return __uint_as_float(__builtin_amdgcn_raw_buffer_load_b32(r, voff, soff, 16)); }
DI void cstore(v4u v, rsrc_t r, u32 voff) { __builtin_amdgcn_raw_buffer_store_b128(v, r, voff, 0, 16); }
DI void cstore1(float v, rsrc_t r, u32 voff) { __builtin_amdgcn_raw_buffer_store_b32(__float_as_uint(v), r, voff, 0, 16); }
DI u32 touch(rsrc_t r, u32 voff) { return __builtin_amdgcn_raw_buffer_load_b32(r, voff, 0, 16); }
#define KEEP(x) asm volatile("" :: "v"(x))
DI void bar() { __syncthreads(); }

constexpr int SPIN_CAP = 1 << 17;
DI void poll(const char* ws, u32 off, bool active, u64 want) {
  GAS u64* p = (GAS u64*)(ws + off);
  for (int sp = 0; sp < SPIN_CAP; ++sp) {
    const u64 f = active ? __hip_atomic_load(p, RLX_AGENT) : want;
    if (__all(f == want)) break;
  }
}
DI void poll256(const char* ws, u32 off, int lane, u64 want) {
  GAS u64* p = (GAS u64*)(ws + off) + lane;
  for (int sp = 0; sp < SPIN_CAP; ++sp) {
    const u64 f0 = __hip_atomic_load(p, RLX_AGENT), f1 = __hip_atomic_load(p + 64, RLX_AGENT), f2 = __hip_atomic_load(p + 128, RLX_AGENT), f3 = __hip_atomic_load(p + 192, RLX_AGENT);
    if (__all(f0 == want && f1 == want && f2 == want && f3 == want)) break;
  }
}
DI bool all_tag(v4u f0, v4u f1, u64 tag) {
  const u32 tl = (u32)tag, th = (u32)(tag >> 32);
  return __all(f0[0] == tl && f0[1] == th && f0[2] == tl && f0[3] == th && f1[0] == tl && f1[1] == th && f1[2] == tl && f1[3] == th);
}
DI void post(char* ws, u32 off, u64 tag) { __hip_atomic_store((GAS u64*)(ws + off), tag, RLX_AGENT); }

struct Args { const u16 *q, *k, *v, *sh, *sk, *sD, *w1, *w2, *w3, *w4, *c, *wm; u16* out; char* ws; };

DI void ln_reduce(const float (&v)[8], float& mean, float& var) {
  const float a0 = tr32(v[0], v[4]), a1 = tr32(v[1], v[5]), a2 = tr32(v[2], v[6]), a3 = tr32(v[3], v[7]);
  mean = red16(tr16(a0, a2)) * (1.f / D);
  var = red16(tr16(a1, a3)) * (1.f / D) - mean * mean;
}
DI v2f ln_coef(float mean, float var) { const float r = __builtin_amdgcn_rsqf(fmaxf(var, 0.f) + 1e-5f); return (v2f){r, -mean * r}; }
DI v4u to_piece(u32 a0, u32 a1, u32 b0, u32 b1) {
  auto r0 = __builtin_amdgcn_permlane32_swap(a0, b0, false, false);
  auto r1 = __builtin_amdgcn_permlane32_swap(a1, b1, false, false);
  return (v4u){r0[0], r1[0], r0[1], r1[1]};
}

struct TileUnit { v4u tl[2]; u32 dst, str, cs; };
template <bool EARLY>
DI void tile_load(const Args& a, int u, bool valid, int lane, TileUnit& t) {
  const bool first = u < (EARLY ? 256 : 896);
  const u64 pa = (u64)(EARLY ? a.w1 : a.w3), pb = (u64)(EARLY ? a.w2 : a.w4);
  const u16* W = (const u16*)(pb + (first ? pa - pb : 0));
  const bool w3 = !EARLY && first;
  const int N = w3 ? F : D;
  const int ur = first ? u : u - (EARLY ? 256 : 896);
  const int s = w3 ? ur & 31 : ur >> 3, np = w3 ? ur >> 5 : ur & 7, nks2 = (EARLY || first) ? 16 : 56;
  t.str = (u32)nks2 * 1024;
  t.dst = valid ? (EARLY ? (first ? OFF_W1 : OFF_W2) : (first ? OFF_W3 : OFF_W4)) + (u32)(4 * np * nks2 + (s >> 1)) * 1024 + (s & 1) * 512 : OOB;
  t.cs = (valid && first) ? OFF_CSP + (u32)(s * CSW + (EARLY ? 0 : D) + 64 * np) * 4 : OOB;
  const rsrc_t rw = mk_rsrc(W, valid ? (u32)(D * (EARLY ? D : F) * 2) : 0u);
  const int jj = lane >> 3;
  const bool o1 = EARLY && first;
  const int rowl = o1 ? 128 * (((np >> 1) + (jj >> 1)) & 3) + (jj & 1) : 8 * (jj >> 2) + (jj & 3);
  const u32 vo = (u32)((rowl * N + 8 * (lane & 7)) * 2), so = (u32)(((o1 ? 4 : 16) * s * N + 64 * np) * 2), ho = (u32)((o1 ? 2 : 4) * N * 2);
  t.tl[0] = bload(rw, vo, so); t.tl[1] = bload(rw, vo, so + ho);
}
DI void tile_store(const TileUnit& t, char* tb, const rsrc_t rws, int lane) {
#pragma unroll
  for (int hh = 0; hh < 2; ++hh) *(v4u*)(tb + hh * 1024 + lane * 16) = t.tl[hh];
  const LAS char* ra = (const LAS char*)tb + (8 * ((lane >> 4) & 1) + ((lane & 15) >> 2)) * 128 + (lane >> 5) * 32 + (lane & 3) * 8;
  float cs[2];
#pragma unroll
  for (int n2 = 0; n2 < 2; ++n2) {
    const v2u lo = __builtin_bit_cast(v2u, __builtin_amdgcn_ds_read_tr16_b64_v4i16((LAS v4s*)(ra + n2 * 64)));
    const v2u hi = __builtin_bit_cast(v2u, __builtin_amdgcn_ds_read_tr16_b64_v4i16((LAS v4s*)(ra + n2 * 64 + 512)));
    cstore((v4u){lo[0], lo[1], hi[0], hi[1]}, rws, t.dst + (2 * n2 + (lane >> 5)) * t.str + (lane & 31) * 16);
    cs[n2] = dot2(lo[0], ONES, dot2(lo[1], ONES, dot2(hi[0], ONES, dot2(hi[1], ONES, 0.f))));
  }
  cstore1(tr16(cs[0], cs[1]), rws, t.cs + (u32)(32 * ((lane >> 4) & 1) + 16 * (lane >> 5) + (lane & 15)) * 4);
}
DI void conv_load_a(v4u (&A)[4][8], const char* lds, int w, int lane) {
  const int ch = w & 1, lh = w >> 2, m = lane & 15, kg = lane >> 4;
  const LAS char* hbase = (const LAS char*)lds + L_HC + ch * 4 * HC_STRIDE + (m & 3) * HC_STRIDE + 2 * (8 * kg + (m & 12)) + lh * 4 * 64;
#pragma unroll
  for (int lp = 0; lp < 4; ++lp)
#pragma unroll
    for (int j = 0; j < 8; ++j) {
      const v2u x0 = *(const volatile LAS v2u*)(hbase + lp * 64 + j * 512), x1 = *(const volatile LAS v2u*)(hbase + lp * 64 + j * 512 + 8);
      A[lp][j] = (v4u){x0[0], x0[1], x1[0], x1[1]};
    }
  SB();
}
DI bool arrive(char* lds, int slot, u32 n, int lane) {
  u32 arr = 0;
  if (lane == 0) arr = __hip_atomic_fetch_add((LAS u32*)(lds + L_CNT + slot * 4), 1u, __ATOMIC_ACQ_REL, __HIP_MEMORY_SCOPE_WORKGROUP);
  return __builtin_amdgcn_readfirstlane(arr) == n - 1;
}
DI void lds_wait(char* lds, int slot, u32 n) {
  for (int sp = 0; sp < SPIN_CAP && __hip_atomic_load((LAS u32*)(lds + L_CNT + slot * 4), __ATOMIC_ACQUIRE, __HIP_MEMORY_SCOPE_WORKGROUP) < n; ++sp) {}
}
DI void load_scalars(const Args& a, int d0, u64& tag, u32& dd) {
  const char* pe = a.ws + C_E; const u16* pd = a.sD + d0;
  asm volatile("" : "+s"(pe), "+s"(pd));
  tag = *(const __attribute__((address_space(4))) u64*)pe;
  dd = *(const __attribute__((address_space(4))) u32*)pd;
  asm volatile("" : "+s"(tag), "+s"(dd));
  tag += 1;
}
template <bool MOD>
DI void prep(const Args& a, char* lds, const rsrc_t rws, int b, int tid, int lane, int w, int d0, u64& tag, u32& dd, v4u (&A)[4][8]) {
  const int t2 = tid - 256, ch = t2 >> 7, t = t2 & 127, wq = w - 4;
  const rsrc_t rh = mk_rsrc(a.sk, S * D * 2);
  const u32 ho = (u32)((d0 + ch) * 4096 + t * 16);
  bar();
  v4u P[2], Q[2];
#pragma unroll
  for (int j = 0; j < 2; ++j) { P[j] = bload(rh, ho + j * 2048, 0); Q[j] = bload(rh, ho + j * 2048 + 16, 0); }
  const u32 t0 = __builtin_amdgcn_raw_buffer_load_b32(rws, (w == 7 && lane < 48) ? (u32)lane * (MB / 2) : OOB, 0, 0);
  const u32 t1 = __builtin_amdgcn_raw_buffer_load_b32(mk_rsrc(a.out, S * D * 2), (w == 7 && lane < 4) ? (u32)lane * (MB / 2) : OOB, 0, 0);
  SB();
  TileUnit tu;
  v4u mx[8]; u32 cc[8];
  const int mrow = 256 * (b & 1) + 64 * wq + (lane >> 3);
  if (MOD) {
    const rsrc_t rc = mk_rsrc(a.c, D * 2), rm = mk_rsrc(a.wm, D * 3072 * 2);
#pragma unroll
    for (int j = 0; j < 8; ++j) cc[j] = __builtin_amdgcn_raw_buffer_load_b16(rc, (u32)mrow * 2, j * 16, 0);
#pragma unroll
    for (int j = 0; j < 8; ++j) mx[j] = bload(rm, (u32)(mrow * 3072 + 64 * (b >> 1) + 8 * (lane & 7)) * 2, j * 8 * 3072 * 2);
  } else {
    tile_load<true>(a, 4 * (b - 96) + wq, b < 224, lane, tu);
  }
  SB();
  load_scalars(a, d0, tag, dd);
  if (t < 32) *(v4u*)(lds + L_U + ch * U_STRIDE + 4096 + t * 16) = (v4u){0, 0, 0, 0};
#pragma unroll
  for (int j = 0; j < 2; ++j) {
    const u32 a0 = __builtin_amdgcn_alignbit(P[j][1], P[j][0], 16), a1 = __builtin_amdgcn_alignbit(P[j][2], P[j][1], 16), a2 = __builtin_amdgcn_alignbit(P[j][3], P[j][2], 16),
              a3 = __builtin_amdgcn_alignbit(Q[j][0], P[j][3], 16), a4 = __builtin_amdgcn_alignbit(Q[j][1], Q[j][0], 16);
    char* hc = lds + L_HC + ch * 4 * HC_STRIDE + (t + 128 * j + 2) * 16;
    *(v4u*)(hc + 0 * HC_STRIDE) = (v4u){a0, a1, a2, a3};
    *(v4u*)(hc + 1 * HC_STRIDE) = (v4u){P[j][1], P[j][2], P[j][3], Q[j][0]};
    *(v4u*)(hc + 2 * HC_STRIDE) = (v4u){a1, a2, a3, a4};
    *(v4u*)(hc + 3 * HC_STRIDE) = (v4u){P[j][2], P[j][3], Q[j][0], Q[j][1]};
    if (j == 0 && t == 0) {
      char* h0 = lds + L_HC + ch * 4 * HC_STRIDE;
      const v4u z = {0, 0, 0, 0};
#pragma unroll
      for (int c = 0; c < 4; ++c) *(v4u*)(h0 + c * HC_STRIDE) = z;
      *(v4u*)(h0 + 16 + 0 * HC_STRIDE) = (v4u){0, 0, 0, P[0][0] << 16};
      *(v4u*)(h0 + 16 + 1 * HC_STRIDE) = (v4u){0, 0, 0, P[0][0]};
      *(v4u*)(h0 + 16 + 2 * HC_STRIDE) = (v4u){0, 0, P[0][0] << 16, a0};
      *(v4u*)(h0 + 16 + 3 * HC_STRIDE) = (v4u){0, 0, P[0][0], P[0][1]};
    }
  }
  arrive(lds, 5, 4, lane);
  STAMP(1);
  if (MOD) {
    float acc[8] = {};
#pragma unroll
    for (int j = 0; j < 8; ++j) {
      const float c = lo2f(cc[j]); const float s = c * __builtin_amdgcn_rcpf(1.f + __builtin_amdgcn_exp2f(-1.442695041f * c));
      const u32 slo = pk2(s, 0.f), shi = slo << 16;
#pragma unroll
      for (int e = 0; e < 4; ++e) { acc[2 * e] = dot2(mx[j][e], slo, acc[2 * e]); acc[2 * e + 1] = dot2(mx[j][e], shi, acc[2 * e + 1]); }
    }
    const float b0 = tr32(acc[0], acc[4]), b1 = tr32(acc[1], acc[5]), b2 = tr32(acc[2], acc[6]), b3 = tr32(acc[3], acc[7]);
    float e0 = tr16(b0, b2), e1 = tr16(b1, b3);
    e0 += dpp<0x128>(e0); e1 += dpp<0x128>(e1);
    if (!(lane & 8)) *(v2f*)(lds + L_MODRED + wq * 256 + (8 * (lane & 7) + 2 * (lane >> 4)) * 4) = (v2f){e0, e1};
    if (arrive(lds, 0, 4, lane)) {
      const float* mr = (const float*)(lds + L_MODRED);
      cstore1(mr[lane] + mr[64 + lane] + mr[128 + lane] + mr[192 + lane], rws, OFF_MODP + (u32)((b & 1) * 3072 + 64 * (b >> 1) + lane) * 4);
      DRAIN();
      if (lane == 0) post(a.ws, C_FW + b * 8, tag);
    }
    STAMP(2);
    lds_wait(lds, 5, 4);
    conv_load_a(A, lds, w, lane);
  } else {
    tile_store(tu, lds + L_TILE + w * 2048, rws, lane);
    lds_wait(lds, 5, 4);
    conv_load_a(A, lds, w, lane);
    DRAIN();
    if (arrive(lds, 0, 4, lane) && lane == 0) post(a.ws, C_FW + b * 8, tag);
    STAMP(2);
  }
  KEEP(t0); KEEP(t1);
}

__global__ __launch_bounds__(512, 2) void mega(const u16* pq, const u16* pk, const u16* pv, const u16* psh, const u16* psk, const u16* psD, const u16* pw1, const u16* pw2,
                                               const u16* pw3, const u16* pw4, const u16* pc, const u16* pwm, u16* pout, char* pws) {
  __shared__ __attribute__((aligned(16))) char lds[LDS_BYTES];
  asm volatile("" : "+s"(pq), "+s"(pk), "+s"(pv), "+s"(psh), "+s"(psk), "+s"(psD), "+s"(pw1), "+s"(pw2), "+s"(pw3), "+s"(pw4), "+s"(pc), "+s"(pwm), "+s"(pout), "+s"(pws));
  const Args a = {pq, pk, pv, psh, psk, psD, pw1, pw2, pw3, pw4, pc, pwm, pout, pws};
  const int b = blockIdx.x, tid = threadIdx.x, lane = tid & 63, w = __builtin_amdgcn_readfirstlane(tid >> 6);
  const int g = b & 7, bi = b >> 3;
  const rsrc_t rws = mk_rsrc(a.ws, WS_BYTES);
  STAMP_INIT(); STAMP(0);
  u64 tag;
  const int d0 = 64 * g + 2 * bi;
  const int tt = 8 * g + (bi >> 2), cgp = bi & 3;
  const int rg = 4 * g + (bi >> 3), cj = bi & 7;
  const int n = lane & 15, kg = lane >> 4;
  u32 dd;
  v4u A[4][8];
  TileUnit late;

  if (w < 4) {
    const int cg = tid & 7, rp = tid >> 3;
    const int r = 64 * bi + 2 * rp, c0 = 64 * g + 8 * cg;
    const rsrc_t rk = mk_rsrc(a.k, S * D * 2), rv = mk_rsrc(a.v, S * D * 2);
    v4u kk[5], vv[2], sc[4];
#pragma unroll
    for (int j = 0; j < 5; ++j) { const int rr = r - 3 + j; kk[j] = bload(rk, rr >= 0 ? (u32)((rr * D + c0) * 2) : OOB, 0); }
#pragma unroll
    for (int j = 0; j < 2; ++j) vv[j] = bload(rv, (u32)(((r + j) * D + c0) * 2), 0);
#pragma unroll
    for (int j = 0; j < 4; ++j) sc[j] = *(const GAS v4u*)(a.sh + c0 * 4 + 8 * j);
    SB();
    if (tid < 2) *(v4u*)(lds + L_CNT + tid * 16) = (v4u){0, 0, 0, 0};
    bar();
    load_scalars(a, d0, tag, dd);
    if (bi == 0 && tid == 0) post(a.ws, C_CNT + g * FL, 0);
    const int pos = 31 - rp;
    char* tb = lds + L_T + cg * 8 * 128 + ((((pos >> 2) ^ cg) << 2) + (pos & 3)) * 4;
#pragma unroll
    for (int c = 0; c < 8; ++c) {
      const u32 sel = (c & 1) ? 0x03020706u : 0x01000504u;
      u32 pr[4];
#pragma unroll
      for (int j = 0; j < 4; ++j) pr[j] = __builtin_amdgcn_perm(kk[j + 1][c >> 1], kk[j][c >> 1], sel);
      const u32 s01 = sc[c >> 1][(c & 1) * 2], s23 = sc[c >> 1][(c & 1) * 2 + 1];
      const float a0 = dot2(pr[2], s01, dot2(pr[0], s23, 0.f));
      const float a1 = dot2(pr[3], s01, dot2(pr[1], s23, 0.f));
      const float v0 = (c & 1) ? hi2f(vv[0][c >> 1]) : lo2f(vv[0][c >> 1]), v1 = (c & 1) ? hi2f(vv[1][c >> 1]) : lo2f(vv[1][c >> 1]);
      *(u32*)(tb + c * 128) = pk2(a1 * v1, a0 * v0);
    }
    arrive(lds, 6, 4, lane);
    lds_wait(lds, 6, 4);
    STAMP(1);
#pragma unroll
    for (int j = 0; j < 2; ++j) {
      const int ch = (tid >> 3) + 32 * j, ck = tid & 7;
      const v4u o = *(const v4u*)(lds + L_T + ch * 128 + ((ck ^ (ch >> 3)) * 16));
      cstore(o, rws, OFF_KVT + (u32)(64 * g + ch) * 4096 + (u32)(31 - bi) * 128 + ck * 16);
    }
    DRAIN();
    if (arrive(lds, 4, 4, lane) && lane == 0) post(a.ws, C_FA + (g * 32 + bi) * FL, tag);
    const u32 tk0 = touch(rws, lane < 16 ? OFF_KVT + (u32)(64 * g + 8 * w + (lane & 7) + 32 * (lane >> 3)) * 4096 + (u32)(31 - bi) * 128 : OOB);
    STAMP(2);
    lds_wait(lds, 5, 4);
    conv_load_a(A, lds, w, lane);
    KEEP(tk0);
    if (w == 0) {
      poll(a.ws, C_FA + (g * 32 + lane) * FL, lane < 32, tag);
      STAMP(56);
      v4u uu[8];
#pragma unroll
      for (int j = 0; j < 8; ++j) uu[j] = cload(rws, lane * 16, OFF_KVT + (u32)d0 * 4096 + j * 1024);
      SB();
#pragma unroll
      for (int j = 0; j < 8; ++j) *(v4u*)(lds + L_U + (j >> 2) * U_STRIDE + (j & 3) * 1024 + lane * 16) = uu[j];
    }
  } else if (b < 96) prep<true>(a, lds, rws, b, tid, lane, w, d0, tag, dd, A);
  else prep<false>(a, lds, rws, b, tid, lane, w, d0, tag, dd, A);
  STAMP(3);

  u32 qv[4][4];
  v2u mp[12];
  {
    const int ch = w & 1, pset = (w >> 1) & 1, lh = w >> 2;
    const LAS char* ubase = (const LAS char*)lds + L_U + ch * U_STRIDE + 2 * (2032 - 16 * n + 8 * kg) + lh * 4 * 64 - 7 * 512;
    bar();
    STAMP(4);
    v4f acc[4] = {};
    v4u B[8];
#pragma unroll
    for (int j = 0; j < 8; ++j) B[j] = *(const LAS v4u*)(ubase + (7 - j) * 512);
    const v4u fw0 = cload(rws, w < 4 ? lane * 16 : OOB, C_FW), fw1 = cload(rws, w < 4 ? lane * 16 : OOB, C_FW + 1024);
    SB();
    {
      const rsrc_t rq = mk_rsrc(a.q, S * D * 2);
#pragma unroll
      for (int ci = 0; ci < 4; ++ci)
#pragma unroll
        for (int t = 0; t < 4; ++t) qv[ci][t] = __builtin_amdgcn_raw_buffer_load_b32(rq, lane * 4, (u32)((32 * tt + 4 * w + t) * 1024 + ((cgp + ci) & 3) * 256), 0);
    }
    tile_load<false>(a, 7 * b + w - 1, w > 0, lane, late);
    SB();
#define CONV_STEPS(P0, P1, P2, P3) \
    _Pragma("unroll") for (int lp = 0; lp < 4; ++lp) { \
      _Pragma("unroll") for (int qq = 0; qq <= P3; ++qq) { \
        if (qq <= P0) { acc[0] = mfma16(A[lp][P0 - qq], B[qq], acc[0]); PIN(acc[0]); } \
        if (qq <= P1) { acc[1] = mfma16(A[lp][P1 - qq], B[qq], acc[1]); PIN(acc[1]); } \
        if (qq <= P2) { acc[2] = mfma16(A[lp][P2 - qq], B[qq], acc[2]); PIN(acc[2]); } \
        acc[3] = mfma16(A[lp][P3 - qq], B[qq], acc[3]); PIN(acc[3]); \
        SB(); \
        if (lp < 3) { B[qq] = *(const LAS v4u*)(ubase + (lp + 1) * 64 + (7 - qq) * 512); SB(); } \
      } \
    }
    if (pset == 0) { CONV_STEPS(0, 3, 4, 7) } else { CONV_STEPS(1, 2, 5, 6) }
    STAMP(5);
    {
      char* red = lds + L_CRED + (lh * 2 + ch) * 8192;
      const int Ql = 4 * n + kg, Qs = Ql ^ ((Ql >> 3) & 3);
#pragma unroll
      for (int i = 0; i < 4; ++i) {
        const int p = pset == 0 ? (i == 0 ? 0 : i == 1 ? 3 : i == 2 ? 4 : 7) : (i == 0 ? 1 : i == 1 ? 2 : i == 2 ? 5 : 6);
        *(v4f*)(red + p * 1024 + Qs * 16) = acc[i];
      }
    }
    if (w < 4) {
      if (!all_tag(fw0, fw1, tag)) poll256(a.ws, C_FW, lane, tag);
#pragma unroll
      for (int i = 0; i < 12; ++i) mp[i] = cload2(rws, tid * 8, OFF_MODP + (u32)((i & 1) * 3072 + (i >> 1) * D) * 4);
    }
    STAMP(45);
    bar();
    STAMP(46);
    {
      const char* red = lds + L_CRED + (tid ^ ((tid >> 3) & 3)) * 16;
      const v4f y0 = *(const v4f*)red + *(const v4f*)(red + 16384), y1 = *(const v4f*)(red + 8192) + *(const v4f*)(red + 24576);
      const v2u u0 = *(const v2u*)(lds + L_U + (2044 - 4 * tid) * 2), u1 = *(const v2u*)(lds + L_U + U_STRIDE + (2044 - 4 * tid) * 2);
      const u32 D0l = dd & 0xffffu, D0h = dd << 16, D1l = dd >> 16, D1h = dd & 0xffff0000u;
      const v4u o = {pk2(dot2(u0[1], D0h, y0[0]), dot2(u1[1], D1h, y1[0])), pk2(dot2(u0[1], D0l, y0[1]), dot2(u1[1], D1l, y1[1])),
                     pk2(dot2(u0[0], D0h, y0[2]), dot2(u1[0], D1h, y1[2])), pk2(dot2(u0[0], D0l, y0[3]), dot2(u1[0], D1l, y1[3]))};
      cstore(o, rws, OFF_Z + (u32)(tid >> 3) * 32768 + (u32)(d0 >> 1) * 128 + (tid & 7) * 16);
    }
    SB();
    if (w < 4) {
      v2f md[6];
#pragma unroll
      for (int i = 0; i < 6; ++i) { const u32 t = pkv(__builtin_bit_cast(v2f, mp[2 * i]) + __builtin_bit_cast(v2f, mp[2 * i + 1])); md[i] = (v2f){lo2f(t), hi2f(t)}; }
      v2f* V = (v2f*)(lds + L_V) + tid;
      V[0] = md[2] * (1.f + md[1]); V[D / 2] = md[2] * md[0]; V[D] = 1.f + md[4]; V[3 * D / 2] = md[3]; V[2 * D] = md[5];
    }
    STAMP(47);
    DRAIN();
    bar();
  }

  v4u wA[16], wB[16];
  const u32 w12o = (u32)(8 * cgp + w) * 16384;
  const int j8 = bi >> 2, j4 = bi >> 3;
#define LOAD_WA(lo, hi) do { SB(); _Pragma("unroll") for (int s_ = lo; s_ < hi; ++s_) wA[s_] = cload(rws, lane * 16, OFF_W1 + w12o + ((s_ + 2 * j8) & 15) * 1024); SB(); } while (0)
#define LOAD_WB(lo, hi) do { SB(); _Pragma("unroll") for (int s_ = lo; s_ < hi; ++s_) wB[s_] = cload(rws, lane * 16, OFF_W2 + w12o + ((s_ + 2 * j8) & 15) * 1024); SB(); } while (0)
  v2f x[4][4];
  u32 gm[4][4][2];
  v2f Cv[4], Ev[4];
  {
    u64 old = 0;
    if (tid == 448) old = __hip_atomic_fetch_add((GAS u64*)(a.ws + C_CNT + g * FL), 1ull, RLX_AGENT);
    if (w == 7 && __builtin_amdgcn_readfirstlane((u32)old) == 31 && lane < 8) post(a.ws, C_FB + (lane * 8 + g) * FL, tag);
    SB();
    STAMP(6);
    const u32 wvo = w < 7 ? lane * 16 : OOB;
#define LOAD_WA7(lo, hi) do { SB(); _Pragma("unroll") for (int s_ = lo; s_ < hi; ++s_) wA[s_] = cload(rws, wvo, OFF_W1 + w12o + ((s_ + 2 * j8) & 15) * 1024); SB(); } while (0)
    LOAD_WA7(0, 4);
    float st[8] = {}, mean, var;
#pragma unroll
    for (int ci = 0; ci < 4; ++ci)
#pragma unroll
      for (int t = 0; t < 4; ++t) { st[2 * t] = dot2(qv[ci][t], ONES, st[2 * t]); st[2 * t + 1] = dot2(qv[ci][t], qv[ci][t], st[2 * t + 1]); }
#pragma unroll
    for (int i = 0; i < 8; ++i) PIN(st[i]);
    LOAD_WA7(4, 8);
    ln_reduce(st, mean, var);
    const v2f l1 = ln_coef(mean, var);
#pragma unroll
    for (int ci = 0; ci < 4; ++ci) {
      LOAD_WA7(8 + 2 * ci, 10 + 2 * ci);
      const float* Vr = (const float*)(lds + L_V) + 128 * ((cgp + ci) & 3) + 2 * lane;
      const v2f Av = *(const v2f*)Vr, Bv = *(const v2f*)(Vr + D);
      Cv[ci] = *(const v2f*)(Vr + 2 * D); Ev[ci] = *(const v2f*)(Vr + 3 * D);
#pragma unroll
      for (int t = 0; t < 4; ++t) {
        x[ci][t] = (v2f){lo2f(qv[ci][t]), hi2f(qv[ci][t])};
        const v2f gg = pfma(pfma(x[ci][t], bc(rdl(l1[0], 16 * t)), bc(rdl(l1[1], 16 * t))), Av, Bv);
        gm[ci][t][0] = pk2(gg[0], 0.f); gm[ci][t][1] = pk2(0.f, gg[1]); PIN(gm[ci][t][0]); PIN(gm[ci][t][1]); PIN(x[ci][t]);
      }
    }
    SB();
  }
  if (w == 7) poll(a.ws, C_FB + (g * 8 + lane) * FL, lane < 8, tag);
  STAMP(7);
  bar();
  STAMP(8);
  {
    v4u zs[4];
#pragma unroll
    for (int i = 0; i < 4; ++i) zs[i] = cload(rws, tid * 16, OFF_Z + (u32)tt * 32768 + i * 8192);
    SB();
    tile_store(late, lds + L_TILE + w * 2048, rws, lane);
    SB();
#pragma unroll
    for (int i = 0; i < 4; ++i) *(v4u*)(lds + L_ZS + i * 8192 + (tid ^ ((tid >> 4) & 7)) * 16) = zs[i];
    SB();
    if (tid == 0 && b == 0) post(a.ws, C_E, tag);
    if (w == 7) LOAD_WA(0, 16);
    float cp[8];
#pragma unroll
    for (int i = 0; i < 8; ++i) cp[i] = cload1(rws, (u32)((tid >> 7) * 8 * CSW + (tid & 127)) * 4, OFF_CSP + (u32)(i * CSW + 128 * cgp) * 4);
    SB();
    bar();
    STAMP(9);
    v4u z[4];
#pragma unroll
    for (int ci = 0; ci < 4; ++ci) z[ci] = *(const v4u*)(lds + L_ZS + ((cgp + ci) & 3) * 8192 + lane * 128 + ((w ^ ((lane >> 1) & 7)) * 16));
    v2f s1[4], s2[4];
#pragma unroll
    for (int ci = 0; ci < 4; ++ci) {
      LOAD_WB(ci, ci + 1);
#pragma unroll
      for (int t = 0; t < 4; ++t) x[ci][t] = (v2f){dot2(z[ci][t], gm[ci][t][0], x[ci][t][0]), dot2(z[ci][t], gm[ci][t][1], x[ci][t][1])};
#pragma unroll
      for (int t = 0; t < 4; ++t) {
        if (ci == 0) { s1[t] = x[ci][t]; s2[t] = x[ci][t] * x[ci][t]; } else { s1[t] += x[ci][t]; s2[t] = pfma(x[ci][t], x[ci][t], s2[t]); }
        PIN(s1[t]); PIN(s2[t]);
      }
    }
    LOAD_WB(4, 5);
    STAMP(42);
    float st[8], mean, var;
#pragma unroll
    for (int t = 0; t < 4; ++t) { st[2 * t] = s1[t][0] + s1[t][1]; st[2 * t + 1] = s2[t][0] + s2[t][1]; PIN(st[2 * t]); PIN(st[2 * t + 1]); }
    ln_reduce(st, mean, var);
    const v2f l2 = ln_coef(mean, var);
#pragma unroll
    for (int t = 0; t < 4; ++t) *(u32*)(lds + L_XH + (4 * w + t) * 264 + lane * 4) = pkv(x[0][t]);
    STAMP(43);
#pragma unroll
    for (int t = 0; t < 4; ++t) {
      LOAD_WB(5 + t, 6 + t);
      const float rs = rdl(l2[0], 16 * t), mo = rdl(l2[1], 16 * t);
      v4u o;
#pragma unroll
      for (int ci = 0; ci < 4; ++ci) { x[ci][t] = pfma(pfma(x[ci][t], bc(rs), bc(mo)), Cv[ci], Ev[ci]); o[ci] = pkv(x[ci][t]); }
      *(v4u*)(lds + L_X + (((lane >> 1) - 4 * j8) & 31) * 1024 + ((((lane & 1) * 32 + 4 * w + t) ^ (lane & 7)) * 16)) = o;
      *(u32*)(lds + L_XH + 32 * 264 + (4 * w + t) * 264 + lane * 4) = o[0];
    }
    *(float*)(lds + L_ST + tid * 4) = ((cp[0] + cp[1]) + (cp[2] + cp[3])) + ((cp[4] + cp[5]) + (cp[6] + cp[7]));
    VMWAIT(9);
    STAMP(44);
    bar();
    if (tid == 0) post(a.ws, C_FW2 + b * 8, tag);
    STAMP(10);
  }
  const u32 xfo = (u32)((kg >> 1) * 1024 + (kg & 1) * 512 + n * 16);
  const u32 pco = (u32)(((lane >> 4) & 1) * 512 + ((lane & 15) + 16 * (lane >> 5)) * 16);
  u32 tch[2];
  const u32 fro = (u32)tt * 32768 + (u32)(8 * cgp + w) * 1024 + pco;
  {
    v4f acc[2] = {};
    v4u xf[8];
    const char* xb0 = lds + L_X + (kg >> 1) * 1024 + (((kg & 1) * 32 + n) ^ kg) * 16;
    const char* xb1 = lds + L_X + (kg >> 1) * 1024 + (((kg & 1) * 32 + n) ^ (4 + kg)) * 16;
#pragma unroll
    for (int i = 0; i < 8; ++i) xf[i] = *(const v4u*)((((i >> 1) & 1) ? xb1 : xb0) + (i >> 1) * 2048 + (i & 1) * 256);
    float st[8] = {};
    SB();
#pragma unroll
    for (int i = 0; i < 16; ++i) {
      acc[i & 1] = mfma16(wA[i >> 1], xf[i & 7], acc[i & 1]); PIN(acc[i & 1]);
      xf[i & 7] = *(const v4u*)((((i >> 1) & 1) ? xb1 : xb0) + ((i + 8) >> 1) * 2048 + (i & 1) * 256);
#pragma unroll
      for (int e = 0; e < 2; ++e) { const float y = x[i >> 2][i & 3][e]; st[2 * (i & 3)] += y; st[2 * (i & 3) + 1] = __builtin_fmaf(y, y, st[2 * (i & 3) + 1]); }
      PIN(st[2 * (i & 3)]); PIN(st[2 * (i & 3) + 1]);
      if ((i & 3) == 1) LOAD_WB(9 + (i >> 2), 10 + (i >> 2));
      SB();
    }
    STAMP(32);
    float mean, var;
    ln_reduce(st, mean, var);
    if (n == 0) *(v2f*)(lds + L_ST1 + (4 * w + kg) * 8) = (v2f){mean, var};
#pragma unroll
    for (int i = 16; i < 32; ++i) {
      acc[i & 1] = mfma16(wA[i >> 1], xf[i & 7], acc[i & 1]); PIN(acc[i & 1]);
      if (i < 24) xf[i & 7] = *(const v4u*)((((i >> 1) & 1) ? xb1 : xb0) + ((i + 8) >> 1) * 2048 + (i & 1) * 256);
    }
    const char* cp = lds + L_ST + (16 * w + 4 * kg) * 4;
    const v4f cs = (*(const v4f*)cp + *(const v4f*)(cp + 512)) + (*(const v4f*)(cp + 1024) + *(const v4f*)(cp + 1536));
    STAMP(33);
    bar();
    STAMP(34);
    u32 pk[2][2];
#pragma unroll
    for (int tb = 0; tb < 2; ++tb) {
      const v2f s3 = *(const v2f*)(lds + L_ST1 + (16 * tb + n) * 8);
      const Gelu gc = gelu_coef(s3[1]);
      const v4f y = acc[tb] - s3[0] * cs;
      pk[tb][0] = pk2(gelu_s(y[0], gc), gelu_s(y[1], gc));
      pk[tb][1] = pk2(gelu_s(y[2], gc), gelu_s(y[3], gc));
    }
    cstore(to_piece(pk[0][0], pk[0][1], pk[1][0], pk[1][1]), rws, OFF_G + fro);
    STAMP(35);
    if (w == 6) poll256(a.ws, C_FW2, lane, tag);
    DRAIN();
    tch[0] = touch(rws, OFF_G + fro);
    STAMP(36);
    bar();
    if (tid == 448) post(a.ws, C_FG + (tt * 4 + cgp) * FL, tag);
    STAMP(11);
  }
  v4u w3[32];
  const u32 w3o = w < 7 ? OFF_W3 + (u32)(7 * cj + w) * 32768 : OFF_CSP + (u32)(D + 224 * cj) * 4, w3s = w < 7 ? 1024 : CSW * 8, w3m = w < 7 ? 16384 : CSW * 4;
#define LOAD_W3(lo, hi) do { SB(); _Pragma("unroll") for (int s_ = lo; s_ < hi; ++s_) w3[s_] = cload(rws, lane * 16, w3o + (((s_ >> 1) + 4 * j4) & 15) * w3s + (s_ & 1) * w3m); SB(); } while (0)
  if (w < 7) { LOAD_WB(13, 16); LOAD_W3(0, 3); }
  else {
    const int ft = 4 * (tt - 1) + lane;
    poll(a.ws, C_FG + ft * FL, lane < 12 && ft >= 0 && ft < 256, tag);
  }
  bar();
  STAMP(12);
  {
    const int sg = w >> 1;
    const int t = 32 * tt + (lane & 31) + (sg == 0 ? -1 : sg == 1 ? 1 : sg == 2 ? -2 : 2);
    const u32 off = (t >= 0 && t < S) ? (u32)((t >> 5) * 32768 + ((lane >> 5) * 32 + (t & 31)) * 16) : OOB;
    v4u xs[4];
#pragma unroll
    for (int i = 0; i < 4; ++i) xs[i] = cload(rws, off, OFF_G + (u32)(4 * w + i) * 1024);
    LOAD_W3(3, 7);
    v4f acc[2];
#pragma unroll
    for (int tb = 0; tb < 2; ++tb) {
      const v2u hv = *(const v2u*)(lds + L_XH + 32 * 264 + (16 * tb + n) * 264 + (16 * w + 4 * kg) * 2);
      acc[tb] = (v4f){lo2f(hv[0]), hi2f(hv[0]), lo2f(hv[1]), hi2f(hv[1])}; PIN(acc[tb]);
    }
    const char* xh = lds + L_XH + ((lane & 15) + 16 * (lane >> 5)) * 264 + (16 * w + 4 * ((lane >> 4) & 1)) * 2;
    const v2u xa = *(const v2u*)xh, xc = *(const v2u*)(xh + 16);
    cstore((v4u){xa[0], xa[1], xc[0], xc[1]}, rws, OFF_X1 + fro);
    SB();
#pragma unroll
    for (int i = 0; i < 4; ++i) *(v4u*)(lds + L_X + (4 * ((w - j8) & 7) + i) * 1024 + lane * 16) = xs[i];
    KEEP(tch[0]);
    if (w == 7) { LOAD_WB(13, 16); LOAD_W3(0, 3); }
    LOAD_W3(7, 10);
    bar();
    STAMP(13);
    v4u xf[8];
    const char* xb = lds + L_X + xfo;
#pragma unroll
    for (int i = 0; i < 8; ++i) xf[i] = *(const v4u*)(xb + (i >> 1) * 2048 + (i & 1) * 256);
    SB();
#pragma unroll
    for (int i = 0; i < 32; ++i) {
      acc[i & 1] = mfma16(wB[i >> 1], xf[i & 7], acc[i & 1]); PIN(acc[i & 1]);
      if (i < 24) xf[i & 7] = *(const v4u*)(xb + ((i + 8) >> 1) * 2048 + (i & 1) * 256);
      if ((i & 3) == 0 && i < 16) LOAD_W3(10 + (i >> 2), 11 + (i >> 2));
      SB();
    }
    cstore(to_piece(pk2(acc[0][0], acc[0][1]), pk2(acc[0][2], acc[0][3]), pk2(acc[1][0], acc[1][1]), pk2(acc[1][2], acc[1][3])), rws, OFF_X1P + fro);
    STAMP(40);
    DRAIN();
    tch[0] = touch(rws, OFF_X1P + fro); tch[1] = touch(rws, OFF_X1 + fro);
    STAMP(41);
    bar();
    if (tid == 448) post(a.ws, C_FX + (tt * 4 + cgp) * FL, tag);
    STAMP(14);
  }
  v4u w4[28];
  u32 ks[7];
#pragma unroll
  for (int s = 0; s < 7; ++s) { const int t = s + (cj == 7 ? 0 : cj); ks[s] = (u32)(t >= 7 ? t - 7 : t) * 1024; }
  const u32 w4o = OFF_W4 + (u32)(4 * cj * 56 + 7 * w) * 1024;
#define LOAD_W4(idx) do { w4[idx] = cload(rws, lane * 16, w4o + (u32)(((idx) / 7 + j4) & 3) * (56 * 1024) + ks[(idx) % 7]); SB(); } while (0)
  const u32 fo4 = (u32)(w >> 2) * 32768 + (u32)(4 * cj + (w & 3)) * 1024 + (u32)((kg & 1) * 512 + n * 16 + (kg >> 1) * 8);
  v2u x1f[2], xpf[2];
  if (w < 7) LOAD_W3(14, 20);
  else poll(a.ws, C_FX + (rg * 8 + lane) * FL, lane < 8, tag);
  bar();
  STAMP(15);
  {
    v4u xs[8];
#pragma unroll
    for (int i = 0; i < 8; ++i) xs[i] = cload(rws, lane * 16, OFF_X1P + (u32)rg * 65536 + (u32)(8 * w + i) * 1024);
    LOAD_W3(20, 24);
    float s1 = 0, s2 = 0;
#pragma unroll
    for (int i = 0; i < 8; ++i) {
      *(v4u*)(lds + L_X + ((w & 4) * 8 + 8 * ((w - j4) & 3) + i) * 1024 + lane * 16) = xs[i];
#pragma unroll
      for (int e = 0; e < 4; ++e) { s1 = dot2(xs[i][e], ONES, s1); s2 = dot2(xs[i][e], xs[i][e], s2); }
      PIN(s1); PIN(s2);
      SB();
    }
    *(float*)(lds + L_ST + w * 256 + (lane & 31) * 8 + (lane >> 5) * 4) = tr32(s1, s2);
    KEEP(tch[0]); KEEP(tch[1]);
    LOAD_W3(24, 32);
    STAMP(49);
    bar();
    STAMP(16);
#pragma unroll
    for (int tb = 0; tb < 2; ++tb) xpf[tb] = *(const v2u*)(lds + L_X + (w >> 2) * 32768 + ((4 * cj + (w & 3) - 8 * j4) & 31) * 1024 + (fo4 & 1023) + tb * 256);
    if (w == 7) {
      LOAD_W3(14, 20);
      v4f cs = {0, 0, 0, 0};
#pragma unroll
      for (int j = 0; j < 32; ++j) cs += __builtin_bit_cast(v4f, w3[j]);
      *(v4f*)(lds + L_CS3 + lane * 16) = cs;
      if (lane == 0) __hip_atomic_store((LAS u32*)(lds + L_CNT + 12), 1u, __ATOMIC_RELEASE, __HIP_MEMORY_SCOPE_WORKGROUP);
    }
    int w_ = w; asm volatile("" : "+s"(w_));
    if (w_ == 7) {
#pragma unroll
      for (int s = 0; s < 28; ++s) LOAD_W4(s);
    } else {
      if (w < 4) __builtin_amdgcn_s_setprio(1);
      const u32 ho = OFF_H + (u32)(2 * rg) * (112 * 1024) + (u32)(2 * (7 * cj + w)) * 1024 + pco;
      const char* xb = lds + L_X + xfo;
      v4u xf[6];
#pragma unroll
      for (int i = 0; i < 6; ++i) xf[i] = *(const v4u*)(xb + (i >> 1) * 2048 + (i & 1) * 256);
      v4f cs[2];
#pragma unroll
      for (int p = 0; p < 2; ++p) {
        v4f acc[2][2] = {};
#pragma unroll
        for (int i = 0; i < 32; ++i) {
          const int fi = 32 * p + i;
#pragma unroll
          for (int mt = 0; mt < 2; ++mt) { acc[mt][i & 1] = mfma16(w3[(i & ~1) + mt], xf[fi % 6], acc[mt][i & 1]); PIN(acc[mt][i & 1]); }
          if (fi + 6 < 64) xf[fi % 6] = *(const v4u*)(xb + ((fi + 6) >> 1) * 2048 + (i & 1) * 256);
          SB();
          if (p == 0 && i % 3 == 1 && i < 30) LOAD_W4(i / 3);
          if (p == 1 && (i & 1)) LOAD_W4(10 + (i >> 1));
          if (p == 1 && i == 11) {
            VMWAIT(6);
            if (arrive(lds, 1, 7, lane) && lane == 0) post(a.ws, C_FH + (rg * 8 + cj) * FL, tag);
            tch[0] = touch(rws, ho - pco + lane * 32);
            SB();
          }
        }
        if (p == 1) { STAMP(17); } else { STAMP(23); }
        if (p == 0) {
          lds_wait(lds, 3, 1);
#pragma unroll
          for (int mt = 0; mt < 2; ++mt) cs[mt] = *(const v4f*)(lds + L_CS3 + (32 * w + 16 * mt + 4 * kg) * 4);
        }
        float mn[2]; Gelu gc[2];
#pragma unroll
        for (int tb = 0; tb < 2; ++tb) {
          const char* sp = lds + L_ST + p * 1024 + (16 * tb + n) * 8;
          v2f st = (*(const v2f*)sp + *(const v2f*)(sp + 256)) + (*(const v2f*)(sp + 512) + *(const v2f*)(sp + 768));
          st *= (1.f / D);
          mn[tb] = -st[0]; gc[tb] = gelu_coef(st[1] - st[0] * st[0]);
        }
#pragma unroll
        for (int mt = 0; mt < 2; ++mt) {
          u32 pk[2][2];
#pragma unroll
          for (int tb = 0; tb < 2; ++tb)
#pragma unroll
            for (int e = 0; e < 2; ++e)
              pk[tb][e] = pk2(gelu_s(__builtin_fmaf(cs[mt][2 * e], mn[tb], acc[mt][tb][2 * e]), gc[tb]), gelu_s(__builtin_fmaf(cs[mt][2 * e + 1], mn[tb], acc[mt][tb][2 * e + 1]), gc[tb]));
          cstore(to_piece(pk[0][0], pk[0][1], pk[1][0], pk[1][1]), rws, ho + p * 112 * 1024 + mt * 1024);
        }
        SB();
        if (p == 1) { STAMP(27); } else { STAMP(24); }
      }
#pragma unroll
      for (int s = 26; s < 28; ++s) LOAD_W4(s);
      VMWAIT(2);
      if (arrive(lds, 2, 7, lane) && lane == 0) post(a.ws, C_FH + (256 + rg * 8 + cj) * FL, tag);
      tch[1] = touch(rws, ho - pco + 112 * 1024 + lane * 32);
    }
    STAMP(18);
  }
  {
    v4u xf[14];
    const u32 xo = OFF_H + (u32)(2 * rg) * (112 * 1024) + (u32)(14 * w) * 1024;
    poll(a.ws, C_FH + (rg * 8 + w) * FL, lane == 0, tag);
    KEEP(tch[0]); KEEP(tch[1]);
    STAMP(19);
#pragma unroll
    for (int s = 0; s < 14; ++s) xf[s] = cload(rws, xfo + (s & 1) * 256, xo + 2 * ks[s >> 1]);
#pragma unroll
    for (int tb = 0; tb < 2; ++tb) x1f[tb] = cload2(rws, fo4 + tb * 256, OFF_X1 + (u32)rg * 65536);
    SB();
#pragma unroll
    for (int p = 0; p < 2; ++p) {
      v4f acc[4][2] = {};
#pragma unroll
      for (int s = 0; s < 14; ++s) {
#pragma unroll
        for (int mt = 0; mt < 4; ++mt) { acc[mt][s & 1] = mfma16(w4[7 * mt + (s >> 1)], xf[s], acc[mt][s & 1]); PIN(acc[mt][s & 1]); }
        SB();
        if (p == 0 && s == 7) {
          STAMP(52);
          poll(a.ws, C_FH + (256 + rg * 8 + w) * FL, lane == 0, tag);
          STAMP(53);
#pragma unroll
          for (int s2 = 0; s2 < 7; ++s2) xf[s2] = cload(rws, xfo + (s2 & 1) * 256, xo + 112 * 1024 + 2 * ks[s2 >> 1]);
        }
        if (p == 0 && s >= 7) { xf[s] = cload(rws, xfo + (s & 1) * 256, xo + 112 * 1024 + 2 * ks[s >> 1]); SB(); }
      }
      if (p == 1) { STAMP(54); } else { STAMP(55); }
      if (p == 1) {
        lds_wait(lds, 2, 7);
      }
#pragma unroll
      for (int mt = 0; mt < 4; ++mt)
#pragma unroll
        for (int tb = 0; tb < 2; ++tb) *(v4f*)(lds + L_RED4 + ((4 * p + ((mt + j4) & 3)) ^ 4) * 16384 + w * 2048 + tb * 1024 + lane * 16) = acc[mt][tb];
      SB();
      if (p == 0) { STAMP(22); }
    }
    bar();
    STAMP(20);
    const int f0 = 64 * cj + 16 * (w & 3);
    const v4f gb = *(const v4f*)((const float*)(lds + L_V) + 4 * D + f0 + 4 * kg);
    u32 o[2][2];
#pragma unroll
    for (int tb = 0; tb < 2; ++tb) {
      v4f m = {lo2f(xpf[tb][0]), hi2f(xpf[tb][0]), lo2f(xpf[tb][1]), hi2f(xpf[tb][1])};
#pragma unroll
      for (int sw = 0; sw < 8; ++sw) m += *(const v4f*)(lds + L_RED4 + (w ^ 4) * 16384 + sw * 2048 + tb * 1024 + lane * 16);
      o[tb][0] = pk2(lo2f(x1f[tb][0]) + gb[0] * m[0], hi2f(x1f[tb][0]) + gb[1] * m[1]);
      o[tb][1] = pk2(lo2f(x1f[tb][1]) + gb[2] * m[2], hi2f(x1f[tb][1]) + gb[3] * m[3]);
    }
    auto e0 = __builtin_amdgcn_permlane16_swap(o[0][0], o[1][0], false, false);
    auto e1 = __builtin_amdgcn_permlane16_swap(o[0][1], o[1][1], false, false);
    *(GAS v4u*)(a.out + (size_t)(64 * rg + 32 * (w >> 2) + n + 16 * (kg & 1)) * D + f0 + 8 * (kg >> 1)) = (v4u){e0[0], e1[0], e0[1], e1[1]};
  }
}

extern "C" void kernel_launch(void* const* d_in, const int* in_sizes, int n_in, void* d_out, int out_size, void* d_ws, size_t ws_size, hipStream_t stream) {
  mega<<<256, 512, 0, stream>>>((const u16*)d_in[0], (const u16*)d_in[1], (const u16*)d_in[2], (const u16*)d_in[3], (const u16*)d_in[4], (const u16*)d_in[5],
                                (const u16*)d_in[7], (const u16*)d_in[8], (const u16*)d_in[9], (const u16*)d_in[10], (const u16*)d_in[11], (const u16*)d_in[12],
                                (u16*)d_out, (char*)d_ws);
}
```

```cpp
#include <hip/hip_runtime.h>
#include <stdint.h>

typedef unsigned short u16;
typedef unsigned int u32;
typedef unsigned long long u64;
typedef u32 v4u __attribute__((ext_vector_type(4)));
typedef u32 v2u __attribute__((ext_vector_type(2)));
typedef short v4s __attribute__((ext_vector_type(4)));
typedef float v2f __attribute__((ext_vector_type(2)));
typedef float v4f __attribute__((ext_vector_type(4)));
typedef float v16f __attribute__((ext_vector_type(16)));
typedef __bf16 bf8 __attribute__((ext_vector_type(8)));
typedef __bf16 bf2 __attribute__((ext_vector_type(2)));
#define DI __device__ __forceinline__
#define LAS __attribute__((address_space(3)))
#define GAS __attribute__((address_space(1)))
#define SB() __builtin_amdgcn_sched_barrier(0)
#define PIN(x) asm volatile("" : "+v"(x))
#define DRAIN() asm volatile("s_waitcnt vmcnt(0)" ::: "memory")
#define VMWAIT(n) asm volatile("s_waitcnt vmcnt(" #n ")" ::: "memory")
#define RLX_AGENT __ATOMIC_RELAXED, __HIP_MEMORY_SCOPE_AGENT

constexpr int S = 2048, D = 512, F = 1792;
constexpr u32 MB = 1u << 20;
constexpr u32 FL = 128;
constexpr u32 C_E = 0, C_FW = 4096, C_FW2 = 6144, C_CNT = 8192, C_FB = 12288;
constexpr u32 C_FA = 65536, C_FG = C_FA + 256 * FL, C_FX = C_FG + 256 * FL, C_FH = C_FX + 256 * FL;
constexpr u32 OFF_MODP = 256 * 1024;
constexpr u32 OFF_KVT = 1 * MB;
constexpr u32 OFF_Z = 3 * MB;
constexpr u32 OFF_G = 5 * MB;
constexpr u32 OFF_X1 = 7 * MB;
constexpr u32 OFF_X1P = 9 * MB;
constexpr u32 OFF_H = 11 * MB;
constexpr u32 OFF_W1 = 18 * MB;
constexpr u32 OFF_W2 = OFF_W1 + 512 * 1024;
constexpr u32 OFF_W3 = 19 * MB;
constexpr u32 OFF_W4 = 21 * MB;
constexpr u32 WS_BYTES = 24 * MB;
constexpr u32 OOB = 0x80000000u;

constexpr int HC_STRIDE = 4160;
constexpr int L_HC = 0;
constexpr int L_U = 33280;
constexpr int U_STRIDE = 4608;
constexpr int L_T = 43008;
constexpr int L_TILE = 51200;
constexpr int L_CRED = 75776;
constexpr int L_Q = 108544;
constexpr int L_X = 0;
constexpr int L_ZS = 67584;
constexpr int L_STASH = 65536;
constexpr int L_RED4 = 0;
constexpr int L_XH = 131072;
constexpr int L_V = 147968;
constexpr int L_MISC = 158208;
constexpr int L_MODRED = L_MISC;
constexpr int L_CS3 = L_MISC;
constexpr int L_ST = L_MISC + 1024;
constexpr int L_ST1 = L_MISC + 3072;
constexpr int L_CNT = L_MISC + 3328;
constexpr int LDS_BYTES = L_CNT + 48;

#define STAMP(slot) do {} while (0)
#define STAMP_INIT() do {} while (0)
#define CSTAMP(slot) do {} while (0)

DI float lo2f(u32 x) { return __uint_as_float(x << 16); }
DI float hi2f(u32 x) { return __uint_as_float(x & 0xffff0000u); }
DI u32 pk2(float lo, float hi) { v2f t = {lo, hi}; bf2 b = __builtin_convertvector(t, bf2); return __builtin_bit_cast(u32, b); }
DI v4f mfma16(v4u a, v4u b, v4f c) { return __builtin_amdgcn_mfma_f32_16x16x32_bf16(__builtin_bit_cast(bf8, a), __builtin_bit_cast(bf8, b), c, 0, 0, 0); }
DI v16f mfma32(v4u a, v4u b, v16f c) { return __builtin_amdgcn_mfma_f32_32x32x16_bf16(__builtin_bit_cast(bf8, a), __builtin_bit_cast(bf8, b), c, 0, 0, 0); }
template <int CTRL> DI float dpp(float x) { return __builtin_bit_cast(float, __builtin_amdgcn_mov_dpp(__builtin_bit_cast(int, x), CTRL, 0xf, 0xf, true)); }
DI float red16(float x) { x += dpp<0xB1>(x); x += dpp<0x4E>(x); x += dpp<0x141>(x); x += dpp<0x140>(x); return x; }
DI float tr32(float a, float b) { auto r = __builtin_amdgcn_permlane32_swap(__float_as_uint(a), __float_as_uint(b), false, false); return __uint_as_float(r[0]) + __uint_as_float(r[1]); }
DI float tr16(float a, float b) { auto r = __builtin_amdgcn_permlane16_swap(__float_as_uint(a), __float_as_uint(b), false, false); return __uint_as_float(r[0]) + __uint_as_float(r[1]); }
DI float dot2(u32 a, u32 b, float c) { return __builtin_amdgcn_fdot2_f32_bf16(__builtin_bit_cast(bf2, a), __builtin_bit_cast(bf2, b), c, false); }
DI float rdl(float x, int l) { return __builtin_bit_cast(float, __builtin_amdgcn_readlane(__builtin_bit_cast(int, x), l)); }
struct Gelu { float k1, k2, irs; };
DI Gelu gelu_coef(float var) {
  const float v = fmaxf(var, 0.f) + 1e-5f, rs = __builtin_amdgcn_rsqf(v);
  return Gelu{-2.302208198f * rs, -0.1029432397f * rs * rs * rs, v * rs};
}
DI float gelu_s(float a, const Gelu& c) {
  const float e = __builtin_amdgcn_exp2f(a * __builtin_fmaf(a * a, c.k2, c.k1));
  float y = a * __builtin_amdgcn_rcpf(__builtin_fmaf(e, c.irs, c.irs));
  asm("" : "+v"(y));
  return y;
}
DI v2f pfma(v2f a, v2f b, v2f c) { return __builtin_elementwise_fma(a, b, c); }
DI v2f bc(float x) { return (v2f){x, x}; }
DI u32 pkv(v2f t) { bf2 b = __builtin_convertvector(t, bf2); return __builtin_bit_cast(u32, b); }
constexpr u32 ONES = 0x3f803f80u;
typedef __amdgpu_buffer_rsrc_t rsrc_t;
DI rsrc_t mk_rsrc(const void* p, u32 bytes) { return __builtin_amdgcn_make_buffer_rsrc((void*)p, 0, bytes, 0x00020000); }
DI v4u bload(rsrc_t r, u32 voff, u32 soff) { return __builtin_amdgcn_raw_buffer_load_b128(r, voff, soff, 0); }
DI v4u cload(rsrc_t r, u32 voff, u32 soff) { return __builtin_amdgcn_raw_buffer_load_b128(r, voff, soff, 16); }
DI v2u cload2(rsrc_t r, u32 voff, u32 soff) { return __builtin_amdgcn_raw_buffer_load_b64(r, voff, soff, 16); }
DI float cload1(rsrc_t r, u32 voff, u32 soff) { return __uint_as_float(__builtin_amdgcn_raw_buffer_load_b32(r, voff, soff, 16)); }
DI void cstore(v4u v, rsrc_t r, u32 voff) { __builtin_amdgcn_raw_buffer_store_b128(v, r, voff, 0, 16); }
DI void cstore1(float v, rsrc_t r, u32 voff) { __builtin_amdgcn_raw_buffer_store_b32(__float_as_uint(v), r, voff, 0, 16); }
DI u32 touch(rsrc_t r, u32 voff) { return __builtin_amdgcn_raw_buffer_load_b32(r, voff, 0, 16); }
#define KEEP(x) asm volatile("" :: "v"(x))
DI void bar() { __syncthreads(); }
template <int V> struct IC { static constexpr int value = V; };
template <int N, int I = 0, class Fn> DI void static_for(Fn&& f) { if constexpr (I < N) { f(IC<I>{}); static_for<N, I + 1>(f); } }

constexpr int SPIN_CAP = 1 << 17;
DI void poll(const char* ws, u32 off, bool active, u64 want) {
  GAS u64* p = (GAS u64*)(ws + off);
  for (int sp = 0; sp < SPIN_CAP; ++sp) {
    const u64 f = active ? __hip_atomic_load(p, RLX_AGENT) : want;
    if (__all(f == want)) break;
  }
}
DI void poll256(const char* ws, u32 off, int lane, u64 want) {
  GAS u64* p = (GAS u64*)(ws + off) + lane;
  for (int sp = 0; sp < SPIN_CAP; ++sp) {
    const u64 f0 = __hip_atomic_load(p, RLX_AGENT), f1 = __hip_atomic_load(p + 64, RLX_AGENT), f2 = __hip_atomic_load(p + 128, RLX_AGENT), f3 = __hip_atomic_load(p + 192, RLX_AGENT);
    if (__all(f0 == want && f1 == want && f2 == want && f3 == want)) break;
  }
}
DI bool all_tag(v4u f0, v4u f1, u64 tag) {
  const u32 tl = (u32)tag, th = (u32)(tag >> 32);
  return __all(f0[0] == tl && f0[1] == th && f0[2] == tl && f0[3] == th && f1[0] == tl && f1[1] == th && f1[2] == tl && f1[3] == th);
}
DI void post(char* ws, u32 off, u64 tag) { __hip_atomic_store((GAS u64*)(ws + off), tag, RLX_AGENT); }

struct Args { const u16 *q, *k, *v, *sh, *sk, *sD, *w1, *w2, *w3, *w4, *c, *wm; u16* out; char* ws; };

DI void ln_reduce(const float (&v)[8], float& mean, float& var) {
  const float a0 = tr32(v[0], v[4]), a1 = tr32(v[1], v[5]), a2 = tr32(v[2], v[6]), a3 = tr32(v[3], v[7]);
  mean = red16(tr16(a0, a2)) * (1.f / D);
  var = red16(tr16(a1, a3)) * (1.f / D) - mean * mean;
}
DI v2f ln_coef(float mean, float var) { const float r = __builtin_amdgcn_rsqf(fmaxf(var, 0.f) + 1e-5f); return (v2f){r, -mean * r}; }
DI v4u to_piece(u32 a0, u32 a1, u32 b0, u32 b1) {
  auto r0 = __builtin_amdgcn_permlane32_swap(a0, b0, false, false);
  auto r1 = __builtin_amdgcn_permlane32_swap(a1, b1, false, false);
  return (v4u){r0[0], r1[0], r0[1], r1[1]};
}

struct TileUnit { v4u tl[2]; u32 dst, str; bool wide; };
template <bool EARLY>
DI void tile_load(const Args& a, int u, bool valid, int lane, TileUnit& t) {
  const bool first = u < (EARLY ? 256 : 896);
  const u64 pa = (u64)(EARLY ? a.w1 : a.w3), pb = (u64)(EARLY ? a.w2 : a.w4);
  const u16* W = (const u16*)(pb + (first ? pa - pb : 0));
  const bool w3 = !EARLY && first;
  const int N = w3 ? F : D;
  const int ur = first ? u : u - (EARLY ? 256 : 896);
  const int s = w3 ? ur & 31 : ur >> 3, np = w3 ? ur >> 5 : ur & 7, nks2 = (EARLY || first) ? 16 : 56;
  t.str = (u32)nks2 * 1024;
  t.wide = w3;
  t.dst = valid ? (EARLY ? (first ? OFF_W1 : OFF_W2) : (first ? OFF_W3 : OFF_W4)) + (u32)(4 * np * nks2) * 1024 + (u32)s * (w3 ? 1024 : 512) : OOB;
  const rsrc_t rw = mk_rsrc(W, valid ? (u32)(D * (EARLY ? D : F) * 2) : 0u);
  const int jj = lane >> 3;
  const bool o1 = EARLY && first;
  const int rowl = o1 ? 128 * (((np >> 1) + (jj >> 1)) & 3) + (jj & 1) : 8 * (jj >> 2) + (jj & 3);
  const u32 vo = (u32)((rowl * N + 8 * (lane & 7)) * 2), so = (u32)(((o1 ? 4 : 16) * s * N + 64 * np) * 2), ho = (u32)((o1 ? 2 : 4) * N * 2);
  t.tl[0] = bload(rw, vo, so); t.tl[1] = bload(rw, vo, so + ho);
}
DI void tile_store(const TileUnit& t, char* tb, const rsrc_t rws, int lane) {
#pragma unroll
  for (int hh = 0; hh < 2; ++hh) *(v4u*)(tb + hh * 1024 + lane * 16) = t.tl[hh];
  const int hb = t.wide ? lane >> 5 : (lane >> 4) & 1, cb = t.wide ? (lane >> 4) & 1 : lane >> 5;
  const LAS char* ra = (const LAS char*)tb + (8 * hb + ((lane & 15) >> 2)) * 128 + cb * 32 + (lane & 3) * 8;
  const u32 lo_ = t.wide ? lane * 16 : (lane >> 5) * t.str + (lane & 31) * 16;
#pragma unroll
  for (int n2 = 0; n2 < 2; ++n2) {
    const v2u lo = __builtin_bit_cast(v2u, __builtin_amdgcn_ds_read_tr16_b64_v4i16((LAS v4s*)(ra + n2 * 64)));
    const v2u hi = __builtin_bit_cast(v2u, __builtin_amdgcn_ds_read_tr16_b64_v4i16((LAS v4s*)(ra + n2 * 64 + 512)));
    cstore((v4u){lo[0], lo[1], hi[0], hi[1]}, rws, t.dst + 2 * n2 * t.str + lo_);
  }
}
DI void conv_load_a(v4u (&A)[4][8], const char* lds, int w, int lane) {
  const int ch = w & 1, lh = w >> 2, m = lane & 15, kg = lane >> 4;
  const LAS char* hbase = (const LAS char*)lds + L_HC + ch * 4 * HC_STRIDE + (m & 3) * HC_STRIDE + 2 * (8 * kg + (m & 12)) + lh * 4 * 64;
#pragma unroll
  for (int lp = 0; lp < 4; ++lp)
#pragma unroll
    for (int j = 0; j < 8; ++j) {
      const v2u x0 = *(const volatile LAS v2u*)(hbase + lp * 64 + j * 512), x1 = *(const volatile LAS v2u*)(hbase + lp * 64 + j * 512 + 8);
      A[lp][j] = (v4u){x0[0], x0[1], x1[0], x1[1]};
    }
  SB();
}
DI bool arrive(char* lds, int slot, u32 n, int lane) {
  u32 arr = 0;
  if (lane == 0) arr = __hip_atomic_fetch_add((LAS u32*)(lds + L_CNT + slot * 4), 1u, __ATOMIC_ACQ_REL, __HIP_MEMORY_SCOPE_WORKGROUP);
  return __builtin_amdgcn_readfirstlane(arr) == n - 1;
}
DI void lds_wait(char* lds, int slot, u32 n) {
  for (int sp = 0; sp < SPIN_CAP && __hip_atomic_load((LAS u32*)(lds + L_CNT + slot * 4), __ATOMIC_ACQUIRE, __HIP_MEMORY_SCOPE_WORKGROUP) < n; ++sp) {}
}
DI void load_scalars(const Args& a, int d0, u64& tag, u32& dd) {
  const char* pe = a.ws + C_E; const u16* pd = a.sD + d0;
  asm volatile("" : "+s"(pe), "+s"(pd));
  tag = *(const __attribute__((address_space(4))) u64*)pe;
  dd = *(const __attribute__((address_space(4))) u32*)pd;
  asm volatile("" : "+s"(tag), "+s"(dd));
  tag += 1;
}
template <bool MOD>
DI void prep(const Args& a, char* lds, const rsrc_t rws, int b, int tid, int lane, int w, int d0, u64& tag, u32& dd, v4u (&A)[4][8]) {
  const int t2 = tid - 256, ch = t2 >> 7, t = t2 & 127, wq = w - 4;
  const rsrc_t rh = mk_rsrc(a.sk, S * D * 2);
  const u32 ho = (u32)((d0 + ch) * 4096 + t * 16);
  bar();
  v4u P[2], Q[2];
#pragma unroll
  for (int j = 0; j < 2; ++j) { P[j] = bload(rh, ho + j * 2048, 0); Q[j] = bload(rh, ho + j * 2048 + 16, 0); }
  SB();
#define PREP_AFTER_H 0
  TileUnit tu[2];
  v4u mx[4]; u32 cc[4];
  const int mrow = 128 * (b & 3) + 32 * wq + (lane >> 3);
  auto prep_loads = [&]() __attribute__((always_inline)) {
  if (MOD) {
    const rsrc_t rc = mk_rsrc(a.c, D * 2), rm = mk_rsrc(a.wm, D * 3072 * 2);
#pragma unroll
    for (int j = 0; j < 4; ++j) cc[j] = __builtin_amdgcn_raw_buffer_load_b16(rc, (u32)mrow * 2, j * 16, 0);
#pragma unroll
    for (int j = 0; j < 4; ++j) mx[j] = bload(rm, (u32)(mrow * 3072 + 64 * (b >> 2) + 8 * (lane & 7)) * 2, j * 8 * 3072 * 2);
  } else {
#pragma unroll
    for (int j = 0; j < 2; ++j) tile_load<true>(a, 8 * (b - 192) + 2 * wq + j, true, lane, tu[j]);
  }
  SB();
  };
  if (!PREP_AFTER_H) prep_loads();
  load_scalars(a, d0, tag, dd);
  if (t < 32) *(v4u*)(lds + L_U + ch * U_STRIDE + 4096 + t * 16) = (v4u){0, 0, 0, 0};
#pragma unroll
  for (int j = 0; j < 2; ++j) {
    const u32 a0 = __builtin_amdgcn_alignbit(P[j][1], P[j][0], 16), a1 = __builtin_amdgcn_alignbit(P[j][2], P[j][1], 16), a2 = __builtin_amdgcn_alignbit(P[j][3], P[j][2], 16),
              a3 = __builtin_amdgcn_alignbit(Q[j][0], P[j][3], 16), a4 = __builtin_amdgcn_alignbit(Q[j][1], Q[j][0], 16);
    char* hc = lds + L_HC + ch * 4 * HC_STRIDE + (t + 128 * j + 2) * 16;
    *(v4u*)(hc + 0 * HC_STRIDE) = (v4u){a0, a1, a2, a3};
    *(v4u*)(hc + 1 * HC_STRIDE) = (v4u){P[j][1], P[j][2], P[j][3], Q[j][0]};
    *(v4u*)(hc + 2 * HC_STRIDE) = (v4u){a1, a2, a3, a4};
    *(v4u*)(hc + 3 * HC_STRIDE) = (v4u){P[j][2], P[j][3], Q[j][0], Q[j][1]};
    if (j == 0 && t == 0) {
      char* h0 = lds + L_HC + ch * 4 * HC_STRIDE;
      const v4u z = {0, 0, 0, 0};
#pragma unroll
      for (int c = 0; c < 4; ++c) *(v4u*)(h0 + c * HC_STRIDE) = z;
      *(v4u*)(h0 + 16 + 0 * HC_STRIDE) = (v4u){0, 0, 0, P[0][0] << 16};
      *(v4u*)(h0 + 16 + 1 * HC_STRIDE) = (v4u){0, 0, 0, P[0][0]};
      *(v4u*)(h0 + 16 + 2 * HC_STRIDE) = (v4u){0, 0, P[0][0] << 16, a0};
      *(v4u*)(h0 + 16 + 3 * HC_STRIDE) = (v4u){0, 0, P[0][0], P[0][1]};
    }
  }
  arrive(lds, 5, 4, lane);
  STAMP(1);
  if (PREP_AFTER_H) { lds_wait(lds, 4, 4); prep_loads(); }
  if (MOD) {
    float acc[8] = {};
#pragma unroll
    for (int j = 0; j < 4; ++j) {
      const float c = lo2f(cc[j]); const float s = c * __builtin_amdgcn_rcpf(1.f + __builtin_amdgcn_exp2f(-1.442695041f * c));
      const u32 slo = pk2(s, 0.f), shi = slo << 16;
#pragma unroll
      for (int e = 0; e < 4; ++e) { acc[2 * e] = dot2(mx[j][e], slo, acc[2 * e]); acc[2 * e + 1] = dot2(mx[j][e], shi, acc[2 * e + 1]); }
    }
    const float b0 = tr32(acc[0], acc[4]), b1 = tr32(acc[1], acc[5]), b2 = tr32(acc[2], acc[6]), b3 = tr32(acc[3], acc[7]);
    float e0 = tr16(b0, b2), e1 = tr16(b1, b3);
    e0 += dpp<0x128>(e0); e1 += dpp<0x128>(e1);
    if (!(lane & 8)) *(v2f*)(lds + L_MODRED + wq * 256 + (8 * (lane & 7) + 2 * (lane >> 4)) * 4) = (v2f){e0, e1};
    if (arrive(lds, 0, 4, lane)) {
      const float* mr = (const float*)(lds + L_MODRED);
      cstore1(mr[lane] + mr[64 + lane] + mr[128 + lane] + mr[192 + lane], rws, OFF_MODP + (u32)((b & 3) * 3072 + 64 * (b >> 2) + lane) * 4);
      DRAIN();
      if (lane == 0) post(a.ws, C_FW + b * 8, tag);
    }
    STAMP(2);
    lds_wait(lds, 5, 4);
    conv_load_a(A, lds, w, lane);
  } else {
#pragma unroll
    for (int j = 0; j < 2; ++j) tile_store(tu[j], lds + L_TILE + (2 * wq + j) * 2048, rws, lane);
    lds_wait(lds, 5, 4);
    conv_load_a(A, lds, w, lane);
    DRAIN();
    if (arrive(lds, 0, 4, lane) && lane == 0) post(a.ws, C_FW + b * 8, tag);
    STAMP(2);
  }
}

__global__ __launch_bounds__(512, 2) void mega(const u16* pq, const u16* pk, const u16* pv, const u16* psh, const u16* psk, const u16* psD, const u16* pw1, const u16* pw2,
                                               const u16* pw3, const u16* pw4, const u16* pc, const u16* pwm, u16* pout, char* pws) {
  __shared__ __attribute__((aligned(16))) char lds[LDS_BYTES];
  asm volatile("" : "+s"(pq), "+s"(pk), "+s"(pv), "+s"(psh), "+s"(psk), "+s"(psD), "+s"(pw1), "+s"(pw2), "+s"(pw3), "+s"(pw4), "+s"(pc), "+s"(pwm), "+s"(pout), "+s"(pws));
  const Args a = {pq, pk, pv, psh, psk, psD, pw1, pw2, pw3, pw4, pc, pwm, pout, pws};
  const int b = blockIdx.x, tid = threadIdx.x, lane = tid & 63, w = __builtin_amdgcn_readfirstlane(tid >> 6);
  const int g = b & 7, bi = b >> 3;
  const rsrc_t rws = mk_rsrc(a.ws, WS_BYTES);
  STAMP_INIT(); STAMP(0);
  u64 tag;
  const int d0 = 64 * g + 2 * bi;
  const int tt = 8 * g + (bi >> 2), cgp = bi & 3;
  const int rg = 4 * g + (bi >> 3), cj = bi & 7;
  const int n = lane & 15, kg = lane >> 4;
  u32 dd;
  v4u A[4][8];
  TileUnit late;

  if (w < 4) {
    const int cg = tid & 7, rp = tid >> 3;
    const int r = 64 * bi + 2 * rp, c0 = 64 * g + 8 * cg;
    const rsrc_t rk = mk_rsrc(a.k, S * D * 2), rv = mk_rsrc(a.v, S * D * 2);
    v4u kk[5], vv[2], sc[4];
#pragma unroll
    for (int j = 0; j < 5; ++j) { const int rr = r - 3 + j; kk[j] = bload(rk, rr >= 0 ? (u32)((rr * D + c0) * 2) : OOB, 0); }
#pragma unroll
    for (int j = 0; j < 2; ++j) vv[j] = bload(rv, (u32)(((r + j) * D + c0) * 2), 0);
#pragma unroll
    for (int j = 0; j < 4; ++j) sc[j] = *(const GAS v4u*)(a.sh + c0 * 4 + 8 * j);
    SB();
    if (tid < 3) *(v4u*)(lds + L_CNT + tid * 16) = (v4u){0, 0, 0, 0};
    bar();
    load_scalars(a, d0, tag, dd);
    if (bi == 0 && tid == 0) post(a.ws, C_CNT + g * FL, 0);
    const int pos = 31 - rp;
    char* tb = lds + L_T + cg * 8 * 128 + ((((pos >> 2) ^ cg) << 2) + (pos & 3)) * 4;
#pragma unroll
    for (int c = 0; c < 8; ++c) {
      const u32 sel = (c & 1) ? 0x03020706u : 0x01000504u;
      u32 pr[4];
#pragma unroll
      for (int j = 0; j < 4; ++j) pr[j] = __builtin_amdgcn_perm(kk[j + 1][c >> 1], kk[j][c >> 1], sel);
      const u32 s01 = sc[c >> 1][(c & 1) * 2], s23 = sc[c >> 1][(c & 1) * 2 + 1];
      const float a0 = dot2(pr[2], s01, dot2(pr[0], s23, 0.f));
      const float a1 = dot2(pr[3], s01, dot2(pr[1], s23, 0.f));
      const float v0 = (c & 1) ? hi2f(vv[0][c >> 1]) : lo2f(vv[0][c >> 1]), v1 = (c & 1) ? hi2f(vv[1][c >> 1]) : lo2f(vv[1][c >> 1]);
      *(u32*)(tb + c * 128) = pk2(a1 * v1, a0 * v0);
    }
    arrive(lds, 6, 4, lane);
    lds_wait(lds, 6, 4);
    STAMP(1);
#pragma unroll
    for (int j = 0; j < 2; ++j) {
      const int ch = (tid >> 3) + 32 * j, ck = tid & 7;
      const v4u o = *(const v4u*)(lds + L_T + ch * 128 + ((ck ^ (ch >> 3)) * 16));
      cstore(o, rws, OFF_KVT + (u32)(64 * g + ch) * 4096 + (u32)(31 - bi) * 128 + ck * 16);
    }
    DRAIN();
    if (arrive(lds, 4, 4, lane) && lane == 0) post(a.ws, C_FA + (g * 32 + bi) * FL, tag);
    const u32 tk0 = touch(rws, lane < 16 ? OFF_KVT + (u32)(64 * g + 8 * w + (lane & 7) + 32 * (lane >> 3)) * 4096 + (u32)(31 - bi) * 128 : OOB);
    STAMP(2);
    lds_wait(lds, 5, 4);
    conv_load_a(A, lds, w, lane);
    KEEP(tk0);
    if (w == 0) {
      poll(a.ws, C_FA + (g * 32 + lane) * FL, lane < 32, tag);
      STAMP(56);
      v4u uu[8];
#pragma unroll
      for (int j = 0; j < 8; ++j) uu[j] = cload(rws, lane * 16, OFF_KVT + (u32)d0 * 4096 + j * 1024);
      SB();
#pragma unroll
      for (int j = 0; j < 8; ++j) *(v4u*)(lds + L_U + (j >> 2) * U_STRIDE + (j & 3) * 1024 + lane * 16) = uu[j];
    }
  } else if (b < 192) prep<true>(a, lds, rws, b, tid, lane, w, d0, tag, dd, A);
  else prep<false>(a, lds, rws, b, tid, lane, w, d0, tag, dd, A);
  STAMP(3);

  u32 qv[4][4];
  v2u mp[24];
  {
    const int ch = w & 1, pset = (w >> 1) & 1, lh = w >> 2;
    const LAS char* ubase = (const LAS char*)lds + L_U + ch * U_STRIDE + 2 * (2032 - 16 * n + 8 * kg) + lh * 4 * 64 - 7 * 512;
    bar();
    STAMP(4);
    v4f acc[4] = {};
    v4u B[8];
#pragma unroll
    for (int j = 0; j < 8; ++j) B[j] = *(const LAS v4u*)(ubase + (7 - j) * 512);
    v4u fw0, fw1, qs[4];
    const rsrc_t rq = mk_rsrc(a.q, S * D * 2);
    SB();
#define CONV_STEPS(P0, P1, P2, P3) \
    _Pragma("unroll") for (int lp = 0; lp < 4; ++lp) { \
      _Pragma("unroll") for (int qq = 0; qq <= P3; ++qq) { \
        if (qq <= P0) { acc[0] = mfma16(A[lp][P0 - qq], B[qq], acc[0]); PIN(acc[0]); } \
        if (qq <= P1) { acc[1] = mfma16(A[lp][P1 - qq], B[qq], acc[1]); PIN(acc[1]); } \
        if (qq <= P2) { acc[2] = mfma16(A[lp][P2 - qq], B[qq], acc[2]); PIN(acc[2]); } \
        acc[3] = mfma16(A[lp][P3 - qq], B[qq], acc[3]); PIN(acc[3]); \
        SB(); \
        if (lp < 3) { B[qq] = *(const LAS v4u*)(ubase + (lp + 1) * 64 + (7 - qq) * 512); SB(); } \
        if (lp == 0 && qq < 4) { qs[qq] = bload(rq, lane * 16, (u32)(32 * tt + 4 * w + qq) * 1024); SB(); } \
        if (lp == 2 && qq == 2) { tile_load<false>(a, 7 * b + w, w < 7, lane, late); SB(); } \
        if (lp == 2 && qq == 0) { fw0 = cload(rws, w < 4 ? lane * 16 : OOB, C_FW); fw1 = cload(rws, w < 4 ? lane * 16 : OOB, C_FW + 1024); SB(); } \
      } \
    }
    if (pset == 0) { CONV_STEPS(0, 3, 4, 7) } else { CONV_STEPS(1, 2, 5, 6) }
    STAMP(5);
    {
      char* red = lds + L_CRED + (lh * 2 + ch) * 8192;
      const int Ql = 4 * n + kg, Qs = Ql ^ ((Ql >> 3) & 3);
#pragma unroll
      for (int i = 0; i < 4; ++i) {
        const int p = pset == 0 ? (i == 0 ? 0 : i == 1 ? 3 : i == 2 ? 4 : 7) : (i == 0 ? 1 : i == 1 ? 2 : i == 2 ? 5 : 6);
        *(v4f*)(red + p * 1024 + Qs * 16) = acc[i];
      }
    }
#pragma unroll
    for (int t = 0; t < 4; ++t) *(v4u*)(lds + L_Q + (4 * w + t) * 1024 + lane * 16) = qs[t];
    if (w < 4) {
      if (!all_tag(fw0, fw1, tag)) poll256(a.ws, C_FW, lane, tag);
#pragma unroll
      for (int i = 0; i < 24; ++i) mp[i] = cload2(rws, tid * 8, OFF_MODP + (u32)((i & 3) * 3072 + (i >> 2) * D) * 4);
    }
    STAMP(45);
    bar();
    STAMP(46);
    {
      const char* red = lds + L_CRED + (tid ^ ((tid >> 3) & 3)) * 16;
      const v4f y0 = *(const v4f*)red + *(const v4f*)(red + 16384), y1 = *(const v4f*)(red + 8192) + *(const v4f*)(red + 24576);
      const v2u u0 = *(const v2u*)(lds + L_U + (2044 - 4 * tid) * 2), u1 = *(const v2u*)(lds + L_U + U_STRIDE + (2044 - 4 * tid) * 2);
      const u32 D0l = dd & 0xffffu, D0h = dd << 16, D1l = dd >> 16, D1h = dd & 0xffff0000u;
      const v4u o = {pk2(dot2(u0[1], D0h, y0[0]), dot2(u1[1], D1h, y1[0])), pk2(dot2(u0[1], D0l, y0[1]), dot2(u1[1], D1l, y1[1])),
                     pk2(dot2(u0[0], D0h, y0[2]), dot2(u1[0], D1h, y1[2])), pk2(dot2(u0[0], D0l, y0[3]), dot2(u1[0], D1l, y1[3]))};
      cstore(o, rws, OFF_Z + (u32)(tid >> 3) * 32768 + (u32)(d0 >> 1) * 128 + (tid & 7) * 16);
    }
    SB();
    if (w < 4) {
      v2f md[6];
#pragma unroll
      for (int i = 0; i < 6; ++i) { const u32 t = pkv((__builtin_bit_cast(v2f, mp[4 * i]) + __builtin_bit_cast(v2f, mp[4 * i + 1])) + (__builtin_bit_cast(v2f, mp[4 * i + 2]) + __builtin_bit_cast(v2f, mp[4 * i + 3]))); md[i] = (v2f){lo2f(t), hi2f(t)}; }
      v2f* V = (v2f*)(lds + L_V) + tid;
      V[0] = md[2] * (1.f + md[1]); V[D / 2] = md[2] * md[0]; V[D] = 1.f + md[4]; V[3 * D / 2] = md[3]; V[2 * D] = md[5];
    }
    STAMP(47);
    DRAIN();
    bar();
  }

  v4u wA[16], wB[16];
  const u32 w12o = (u32)(8 * cgp + w) * 16384;
  const int j8 = bi >> 2, j4 = bi >> 3;
#define LOAD_WA(lo, hi) do { SB(); _Pragma("unroll") for (int s_ = lo; s_ < hi; ++s_) wA[s_] = cload(rws, lane * 16, OFF_W1 + w12o + ((s_ + 2 * j8) & 15) * 1024); SB(); } while (0)
#define LOAD_WB(lo, hi) do { SB(); _Pragma("unroll") for (int s_ = lo; s_ < hi; ++s_) wB[s_] = cload(rws, lane * 16, OFF_W2 + w12o + ((s_ + 2 * j8) & 15) * 1024); SB(); } while (0)
  u32 tch[2], wrm[4];
#define WARM(r) do { SB(); wrm[r] = touch(rws, (w < 7 && lane < 32) ? ((r) < 2 ? OFF_W3 : OFF_W4) + (u32)((((r) & 1) * 32 + bi) * 224 + w * 32 + lane) * 128 : OOB); SB(); } while (0)
  v2f x[4][4];
  u32 gm[4][4][2];
  v2f Cv[4], Ev[4];
  {
    u64 old = 0;
    if (tid == 448) old = __hip_atomic_fetch_add((GAS u64*)(a.ws + C_CNT + g * FL), 1ull, RLX_AGENT);
    if (w == 7 && __builtin_amdgcn_readfirstlane((u32)old) == 31 && lane < 8) post(a.ws, C_FB + (lane * 8 + g) * FL, tag);
    SB();
    STAMP(6);
    const u32 wvo = w < 7 ? lane * 16 : OOB;
#define LOAD_WA7(lo, hi) do { SB(); _Pragma("unroll") for (int s_ = lo; s_ < hi; ++s_) wA[s_] = cload(rws, wvo, OFF_W1 + w12o + ((s_ + 2 * j8) & 15) * 1024); SB(); } while (0)
    tile_store(late, lds + L_TILE + w * 2048, rws, lane);
    LOAD_WA7(0, 4);
#pragma unroll
    for (int ci = 0; ci < 4; ++ci)
#pragma unroll
      for (int t = 0; t < 4; ++t) qv[ci][t] = *(const u32*)(lds + L_Q + (4 * w + t) * 1024 + ((cgp + ci) & 3) * 256 + lane * 4);
    float st[8] = {}, mean, var;
#pragma unroll
    for (int ci = 0; ci < 4; ++ci)
#pragma unroll
      for (int t = 0; t < 4; ++t) { st[2 * t] = dot2(qv[ci][t], ONES, st[2 * t]); st[2 * t + 1] = dot2(qv[ci][t], qv[ci][t], st[2 * t + 1]); }
#pragma unroll
    for (int i = 0; i < 8; ++i) PIN(st[i]);
    LOAD_WA7(4, 8);
    ln_reduce(st, mean, var);
    const v2f l1 = ln_coef(mean, var);
#pragma unroll
    for (int ci = 0; ci < 4; ++ci) {
      LOAD_WA7(8 + 2 * ci, 10 + 2 * ci);
      const float* Vr = (const float*)(lds + L_V) + 128 * ((cgp + ci) & 3) + 2 * lane;
      const v2f Av = *(const v2f*)Vr, Bv = *(const v2f*)(Vr + D);
      Cv[ci] = *(const v2f*)(Vr + 2 * D); Ev[ci] = *(const v2f*)(Vr + 3 * D);
#pragma unroll
      for (int t = 0; t < 4; ++t) {
        x[ci][t] = (v2f){lo2f(qv[ci][t]), hi2f(qv[ci][t])};
        const v2f gg = pfma(pfma(x[ci][t], bc(rdl(l1[0], 16 * t)), bc(rdl(l1[1], 16 * t))), Av, Bv);
        gm[ci][t][0] = pk2(gg[0], 0.f); gm[ci][t][1] = pk2(0.f, gg[1]); PIN(gm[ci][t][0]); PIN(gm[ci][t][1]); PIN(x[ci][t]);
      }
    }
    SB();
  }
#pragma unroll
  for (int s = 0; s < 4; ++s) wB[s] = cload(rws, w < 7 ? lane * 16 : OOB, OFF_W2 + w12o + ((s + 2 * j8) & 15) * 1024);
  SB();
  VMWAIT(20);
  if (w == 7) poll(a.ws, C_FB + (g * 8 + lane) * FL, lane < 8, tag);
  STAMP(7);
  bar();
  STAMP(8);
  v4u w3[32];
  const u32 w3o = OFF_W3 + (u32)(7 * cj + w) * 32768, w3v = w < 7 ? lane * 16 : OOB;
#define LOAD_W3(lo, hi) do { SB(); _Pragma("unroll") for (int s_ = lo; s_ < hi; ++s_) w3[s_] = cload(rws, w3v, w3o + ((s_ + 8 * j4) & 31) * 1024); SB(); } while (0)
  const v4u ones = {ONES, ONES, ONES, ONES};
  v4f cs1 = {};
  {
    v4u zs[4];
#pragma unroll
    for (int i = 0; i < 4; ++i) zs[i] = cload(rws, tid * 16, OFF_Z + (u32)tt * 32768 + i * 8192);
    LOAD_WB(4, 7);
    if (tid == 0) { post(a.ws, C_FW2 + b * 8, tag); if (b == 0) post(a.ws, C_E, tag); }
    SB();
#pragma unroll
    for (int i = 0; i < 4; ++i) *(v4u*)(lds + L_ZS + i * 8192 + (tid ^ ((tid >> 4) & 7)) * 16) = zs[i];
    SB();
    if (w == 7) { LOAD_WA(0, 16); LOAD_WB(0, 4); }
    WARM(0);
    bar();
    STAMP(9);
    v4u z[4];
#pragma unroll
    for (int ci = 0; ci < 4; ++ci) z[ci] = *(const v4u*)(lds + L_ZS + ((cgp + ci) & 3) * 8192 + lane * 128 + ((w ^ ((lane >> 1) & 7)) * 16));
    v2f s1[4], s2[4];
#pragma unroll
    for (int ci = 0; ci < 4; ++ci) {
#pragma unroll
      for (int t = 0; t < 4; ++t) x[ci][t] = (v2f){dot2(z[ci][t], gm[ci][t][0], x[ci][t][0]), dot2(z[ci][t], gm[ci][t][1], x[ci][t][1])};
#pragma unroll
      for (int t = 0; t < 4; ++t) {
        if (ci == 0) { s1[t] = x[ci][t]; s2[t] = x[ci][t] * x[ci][t]; } else { s1[t] += x[ci][t]; s2[t] = pfma(x[ci][t], x[ci][t], s2[t]); }
        PIN(s1[t]); PIN(s2[t]);
      }
    }
    STAMP(42);
    float st[8], mean, var;
#pragma unroll
    for (int t = 0; t < 4; ++t) { st[2 * t] = s1[t][0] + s1[t][1]; st[2 * t + 1] = s2[t][0] + s2[t][1]; PIN(st[2 * t]); PIN(st[2 * t + 1]); }
    ln_reduce(st, mean, var);
    const v2f l2 = ln_coef(mean, var);
#pragma unroll
    for (int t = 0; t < 4; ++t) *(u32*)(lds + L_XH + (4 * w + t) * 264 + lane * 4) = pkv(x[0][t]);
    const v4u f20 = cload(rws, w == 0 ? lane * 16 : OOB, C_FW2), f21 = cload(rws, w == 0 ? lane * 16 : OOB, C_FW2 + 1024);
    STAMP(43);
#pragma unroll
    for (int t = 0; t < 4; ++t) {
      const float rs = rdl(l2[0], 16 * t), mo = rdl(l2[1], 16 * t);
      v4u o;
#pragma unroll
      for (int ci = 0; ci < 4; ++ci) { x[ci][t] = pfma(pfma(x[ci][t], bc(rs), bc(mo)), Cv[ci], Ev[ci]); o[ci] = pkv(x[ci][t]); }
      *(v4u*)(lds + L_X + (((lane >> 1) - 4 * j8) & 31) * 1024 + ((((lane & 1) * 32 + 4 * w + t) ^ (lane & 7)) * 16)) = o;
      *(u32*)(lds + L_XH + 32 * 264 + (4 * w + t) * 264 + lane * 4) = o[0];
    }
    SB();
#pragma unroll
    for (int s = 0; s < 16; ++s) cs1 = mfma16(wA[s], ones, cs1);
    PIN(cs1);
    if (w == 0 && !all_tag(f20, f21, tag)) poll256(a.ws, C_FW2, lane, tag);
    WARM(1);
    STAMP(44);
    bar();
    STAMP(10);
  }
  const u32 xfo = (u32)((kg >> 1) * 1024 + (kg & 1) * 512 + n * 16);
  const u32 pco = (u32)(((lane >> 4) & 1) * 512 + ((lane & 15) + 16 * (lane >> 5)) * 16);
  const u32 fro = (u32)tt * 32768 + (u32)(8 * cgp + w) * 1024 + pco;
  {
    v4f acc[2] = {};
    v4u xf[8];
    const char* xb0 = lds + L_X + (kg >> 1) * 1024 + (((kg & 1) * 32 + n) ^ kg) * 16;
    const char* xb1 = lds + L_X + (kg >> 1) * 1024 + (((kg & 1) * 32 + n) ^ (4 + kg)) * 16;
#pragma unroll
    for (int i = 0; i < 8; ++i) xf[i] = *(const v4u*)((((i >> 1) & 1) ? xb1 : xb0) + (i >> 1) * 2048 + (i & 1) * 256);
    float st[8] = {};
    SB();
#pragma unroll
    for (int i = 0; i < 16; ++i) {
      acc[i & 1] = mfma16(wA[i >> 1], xf[i & 7], acc[i & 1]); PIN(acc[i & 1]);
      xf[i & 7] = *(const v4u*)((((i >> 1) & 1) ? xb1 : xb0) + ((i + 8) >> 1) * 2048 + (i & 1) * 256);
#pragma unroll
      for (int e = 0; e < 2; ++e) { const float y = x[i >> 2][i & 3][e]; st[2 * (i & 3)] += y; st[2 * (i & 3) + 1] = __builtin_fmaf(y, y, st[2 * (i & 3) + 1]); }
      PIN(st[2 * (i & 3)]); PIN(st[2 * (i & 3) + 1]);
      SB();
    }
    STAMP(32);
    float mean, var;
    ln_reduce(st, mean, var);
    if (n == 0) *(v2f*)(lds + L_ST1 + (4 * w + kg) * 8) = (v2f){mean, var};
#pragma unroll
    for (int i = 16; i < 32; ++i) {
      acc[i & 1] = mfma16(wA[i >> 1], xf[i & 7], acc[i & 1]); PIN(acc[i & 1]);
      if (i < 24) xf[i & 7] = *(const v4u*)((((i >> 1) & 1) ? xb1 : xb0) + ((i + 8) >> 1) * 2048 + (i & 1) * 256);
    }
    LOAD_WB(7, 10);
    STAMP(33);
    bar();
    STAMP(34);
    u32 pk[2][2];
#pragma unroll
    for (int tb = 0; tb < 2; ++tb) {
      const v2f s3 = *(const v2f*)(lds + L_ST1 + (16 * tb + n) * 8);
      const Gelu gc = gelu_coef(s3[1]);
      const v4f y = acc[tb] - s3[0] * cs1;
      pk[tb][0] = pk2(gelu_s(y[0], gc), gelu_s(y[1], gc));
      pk[tb][1] = pk2(gelu_s(y[2], gc), gelu_s(y[3], gc));
    }
    cstore(to_piece(pk[0][0], pk[0][1], pk[1][0], pk[1][1]), rws, OFF_G + fro);
    STAMP(35);
    DRAIN();
    KEEP(wrm[0]); KEEP(wrm[1]);
    tch[0] = touch(rws, OFF_G + fro);
    STAMP(36);
    bar();
    if (tid == 448) post(a.ws, C_FG + (tt * 4 + cgp) * FL, tag);
    STAMP(11);
  }
  if (w < 7) { LOAD_WB(10, 16); LOAD_W3(0, 5); }
  else {
    const int ft = 4 * (tt - 1) + lane;
    poll(a.ws, C_FG + ft * FL, lane < 12 && ft >= 0 && ft < 256, tag);
  }
  bar();
  STAMP(12);
  {
    const int sg = w >> 1;
    const int t = 32 * tt + (lane & 31) + (sg == 0 ? -1 : sg == 1 ? 1 : sg == 2 ? -2 : 2);
    const u32 off = (t >= 0 && t < S) ? (u32)((t >> 5) * 32768 + ((lane >> 5) * 32 + (t & 31)) * 16) : OOB;
    v4u xs[4];
#pragma unroll
    for (int i = 0; i < 4; ++i) xs[i] = cload(rws, off, OFF_G + (u32)(4 * w + i) * 1024);
    SB();
    v4f acc[2];
#pragma unroll
    for (int tb = 0; tb < 2; ++tb) {
      const v2u hv = *(const v2u*)(lds + L_XH + 32 * 264 + (16 * tb + n) * 264 + (16 * w + 4 * kg) * 2);
      acc[tb] = (v4f){lo2f(hv[0]), hi2f(hv[0]), lo2f(hv[1]), hi2f(hv[1])}; PIN(acc[tb]);
    }
    const char* xh = lds + L_XH + ((lane & 15) + 16 * (lane >> 5)) * 264 + (16 * w + 4 * ((lane >> 4) & 1)) * 2;
    const v2u xa = *(const v2u*)xh, xc = *(const v2u*)(xh + 16);
    SB();
#pragma unroll
    for (int i = 0; i < 4; ++i) *(v4u*)(lds + L_X + (4 * ((w - j8) & 7) + i) * 1024 + lane * 16) = xs[i];
    KEEP(tch[0]);
    if (w == 7) LOAD_WB(10, 16);
    LOAD_W3(5, 9);
    bar();
    STAMP(13);
    v4u xf[8];
    const char* xb = lds + L_X + xfo;
#pragma unroll
    for (int i = 0; i < 8; ++i) xf[i] = *(const v4u*)(xb + (i >> 1) * 2048 + (i & 1) * 256);
    cstore((v4u){xa[0], xa[1], xc[0], xc[1]}, rws, OFF_X1 + fro);
    SB();
#pragma unroll
    for (int i = 0; i < 32; ++i) {
      acc[i & 1] = mfma16(wB[i >> 1], xf[i & 7], acc[i & 1]); PIN(acc[i & 1]);
      if (i < 24) xf[i & 7] = *(const v4u*)(xb + ((i + 8) >> 1) * 2048 + (i & 1) * 256);
      if (i % 3 == 0 && i < 27) LOAD_W3(9 + i / 3, 10 + i / 3);
      SB();
    }
    cstore(to_piece(pk2(acc[0][0], acc[0][1]), pk2(acc[0][2], acc[0][3]), pk2(acc[1][0], acc[1][1]), pk2(acc[1][2], acc[1][3])), rws, OFF_X1P + fro);
    STAMP(40);
    DRAIN();
    tch[0] = touch(rws, OFF_X1P + fro); tch[1] = touch(rws, OFF_X1 + fro);
    STAMP(41);
    bar();
    if (tid == 448) post(a.ws, C_FX + (tt * 4 + cgp) * FL, tag);
    STAMP(14);
  }
  v4u w4[28];
  u32 ks[7];
#pragma unroll
  for (int s = 0; s < 7; ++s) { const int t = s + (cj == 7 ? 0 : cj); ks[s] = (u32)(t >= 7 ? t - 7 : t) * 1024; }
  const u32 w4o = OFF_W4 + (u32)(4 * cj * 56 + 7 * w) * 1024;
#define LOAD_W4(idx) do { w4[idx] = cload(rws, lane * 16, w4o + (u32)(((idx) / 7 + j4) & 3) * (56 * 1024) + ks[(idx) % 7]); SB(); } while (0)
  const u32 fo4 = (u32)(w >> 2) * 32768 + (u32)(4 * cj + (w & 3)) * 1024 + (u32)((kg & 1) * 512 + n * 16 + (kg >> 1) * 8);
  v2u x1f[2], xpf[2];
  if (w < 7) {
    LOAD_W3(18, 32);
    const u32 sv = ((kg & 1) == (n >> 3)) ? ONES : 0u;
    const v4u sel = {sv, sv, sv, sv};
    v4f c0 = {}, c1 = {};
#pragma unroll
    for (int s = 0; s < 32; s += 2) { c0 = mfma16(w3[s], sel, c0); c1 = mfma16(w3[s + 1], sel, c1); }
    if ((n & 7) == 0) *(v4f*)(lds + L_CS3 + w * 128 + (n >> 3) * 64 + kg * 16) = c0 + c1;
  } else poll(a.ws, C_FX + (rg * 8 + lane) * FL, lane < 8, tag);
  bar();
  STAMP(15);
  {
    v4u xs[8];
#pragma unroll
    for (int i = 0; i < 8; ++i) xs[i] = cload(rws, lane * 16, OFF_X1P + (u32)rg * 65536 + (u32)(8 * w + i) * 1024);
    SB();
    float s1 = 0, s2 = 0;
#pragma unroll
    for (int i = 0; i < 8; ++i) {
      *(v4u*)(lds + L_X + ((w & 4) * 8 + 8 * ((w - j4) & 3) + i) * 1024 + lane * 16) = xs[i];
#pragma unroll
      for (int e = 0; e < 4; ++e) { s1 = dot2(xs[i][e], ONES, s1); s2 = dot2(xs[i][e], xs[i][e], s2); }
      PIN(s1); PIN(s2);
      SB();
    }
    *(float*)(lds + L_ST + w * 256 + (lane & 31) * 8 + (lane >> 5) * 4) = tr32(s1, s2);
    KEEP(tch[0]); KEEP(tch[1]);
    LOAD_W4(0); LOAD_W4(1);
    STAMP(49);
    bar();
    STAMP(16);
    v4u xf[14];
    const u32 xo = OFF_H + (u32)(2 * rg) * (112 * 1024) + (u32)(14 * w) * 1024;
#define LOAD_XF(p, lo, hi) do { SB(); _Pragma("unroll") for (int s_ = lo; s_ < hi; ++s_) xf[s_] = cload(rws, xfo + (s_ & 1) * 256, xo + (p) * 112 * 1024 + 2 * ks[s_ >> 1]); SB(); } while (0)
#define READ_XPF() do { _Pragma("unroll") for (int tb = 0; tb < 2; ++tb) { xpf[tb] = *(const v2u*)(lds + L_X + (w >> 2) * 32768 + ((4 * cj + (w & 3) - 8 * j4) & 31) * 1024 + (fo4 & 1023) + tb * 256); PIN(xpf[tb]); } } while (0)
    if (w == 7) {
      READ_XPF();
#pragma unroll
      for (int s = 2; s < 28; ++s) { LOAD_W4(s); __builtin_amdgcn_s_sleep(1); }
      tch[0] = 0; tch[1] = 0;
    } else {
      const u32 ho = OFF_H + (u32)(2 * rg) * (112 * 1024) + (u32)(2 * (7 * cj + w)) * 1024;
      const char* xb = lds + L_X + lane * 16;
      v4u xa[6];
#pragma unroll
      for (int i = 0; i < 6; ++i) xa[i] = *(const v4u*)(xb + (i & 1) * 32768 + (i >> 1) * 1024);
      SB();
      v4f cs[4];
      Gelu gc[2]; float mn[2];
      v16f acc[2] = {};
      static_for<64>([&](auto I) __attribute__((always_inline)) {
        constexpr int fi = I.value, p = fi & 1, i = fi >> 1;
        acc[p] = mfma32(w3[i], xa[fi % 6], acc[p]); PIN(acc[p]);
        if constexpr (fi + 6 < 64) xa[fi % 6] = *(const v4u*)(xb + p * 32768 + ((fi + 6) >> 1) * 1024);
        if constexpr (fi == 40 || fi == 44) {
          constexpr int t = fi == 40 ? 0 : 1;
          const char* sp = lds + L_ST + t * 1024 + (lane & 31) * 8;
          v2f st = (*(const v2f*)sp + *(const v2f*)(sp + 256)) + (*(const v2f*)(sp + 512) + *(const v2f*)(sp + 768));
          st *= (1.f / D);
          gc[t] = gelu_coef(st[1] - st[0] * st[0]); mn[t] = -st[0];
          PIN(gc[t].k1); PIN(gc[t].k2); PIN(gc[t].irs); PIN(mn[t]);
        }
        if constexpr (fi == 58) {
#pragma unroll
          for (int q = 0; q < 4; ++q) cs[q] = *(const v4f*)(lds + L_CS3 + w * 128 + (8 * q + 4 * (lane >> 5)) * 4);
          READ_XPF();
        }
        SB();
        if constexpr (p == 1 && i < 26) LOAD_W4(2 + i);
      });
      STAMP(17);
#pragma unroll
      for (int p = 0; p < 2; ++p)
#pragma unroll
        for (int s = 0; s < 2; ++s) {
          v4u o;
#pragma unroll
          for (int e = 0; e < 4; ++e) {
            const int r = 8 * s + 2 * e;
            o[e] = pk2(gelu_s(__builtin_fmaf(cs[r >> 2][r & 3], mn[p], acc[p][r]), gc[p]), gelu_s(__builtin_fmaf(cs[r >> 2][(r & 3) + 1], mn[p], acc[p][r + 1]), gc[p]));
          }
          cstore(o, rws, ho + p * 112 * 1024 + s * 1024 + lane * 16);
          SB();
        }
      STAMP(27);
      DRAIN();
      tch[0] = touch(rws, ho + lane * 32); tch[1] = touch(rws, ho + 112 * 1024 + lane * 32);
    }
#pragma unroll
    for (int tb = 0; tb < 2; ++tb) x1f[tb] = cload2(rws, fo4 + tb * 256, OFF_X1 + (u32)rg * 65536);
    STAMP(18);
    bar();
    if (w == 7) {
      if (lane == 0) post(a.ws, C_FH + (rg * 8 + cj) * FL, tag);
      poll(a.ws, C_FH + (rg * 8 + lane) * FL, lane < 8, tag);
    }
    bar();
    STAMP(19);
    LOAD_XF(0, 0, 14);
    KEEP(tch[0]); KEEP(tch[1]);
#pragma unroll
    for (int p = 0; p < 2; ++p) {
      v4f acc[4][2] = {};
#pragma unroll
      for (int s = 0; s < 14; ++s) {
#pragma unroll
        for (int mt = 0; mt < 4; ++mt) { acc[mt][s & 1] = mfma16(w4[7 * mt + (s >> 1)], xf[s], acc[mt][s & 1]); PIN(acc[mt][s & 1]); }
        if (p == 0) LOAD_XF(1, s, s + 1);
        SB();
      }
      if (p == 1) { STAMP(54); } else { STAMP(55); }
#pragma unroll
      for (int mt = 0; mt < 4; ++mt)
#pragma unroll
        for (int tb = 0; tb < 2; ++tb) *(v4f*)(lds + L_RED4 + ((4 * p + ((mt + j4) & 3)) ^ 4) * 16384 + w * 2048 + tb * 1024 + lane * 16) = acc[mt][tb];
      SB();
      if (p == 0) { STAMP(22); }
    }
    bar();
    STAMP(20);
    const int f0 = 64 * cj + 16 * (w & 3);
    const v4f gb = *(const v4f*)((const float*)(lds + L_V) + 4 * D + f0 + 4 * kg);
    u32 o[2][2];
#pragma unroll
    for (int tb = 0; tb < 2; ++tb) {
      v4f m = {lo2f(xpf[tb][0]), hi2f(xpf[tb][0]), lo2f(xpf[tb][1]), hi2f(xpf[tb][1])};
#pragma unroll
      for (int sw = 0; sw < 8; ++sw) m += *(const v4f*)(lds + L_RED4 + (w ^ 4) * 16384 + sw * 2048 + tb * 1024 + lane * 16);
      o[tb][0] = pk2(lo2f(x1f[tb][0]) + gb[0] * m[0], hi2f(x1f[tb][0]) + gb[1] * m[1]);
      o[tb][1] = pk2(lo2f(x1f[tb][1]) + gb[2] * m[2], hi2f(x1f[tb][1]) + gb[3] * m[3]);
    }
    auto e0 = __builtin_amdgcn_permlane16_swap(o[0][0], o[1][0], false, false);
    auto e1 = __builtin_amdgcn_permlane16_swap(o[0][1], o[1][1], false, false);
    *(GAS v4u*)(a.out + (size_t)(64 * rg + 32 * (w >> 2) + n + 16 * (kg & 1)) * D + f0 + 8 * (kg >> 1)) = (v4u){e0[0], e1[0], e0[1], e1[1]};
  }
}

extern "C" void kernel_launch(void* const* d_in, const int* in_sizes, int n_in, void* d_out, int out_size, void* d_ws, size_t ws_size, hipStream_t stream) {
  mega<<<256, 512, 0, stream>>>((const u16*)d_in[0], (const u16*)d_in[1], (const u16*)d_in[2], (const u16*)d_in[3], (const u16*)d_in[4], (const u16*)d_in[5],
                                (const u16*)d_in[7], (const u16*)d_in[8], (const u16*)d_in[9], (const u16*)d_in[10], (const u16*)d_in[11], (const u16*)d_in[12],
                                (u16*)d_out, (char*)d_ws);
}
```

```cpp
#include <hip/hip_runtime.h>
#include <stdint.h>

typedef unsigned short u16;
typedef unsigned int u32;
typedef unsigned long long u64;
typedef u32 v4u __attribute__((ext_vector_type(4)));
typedef u32 v2u __attribute__((ext_vector_type(2)));
typedef short v4s __attribute__((ext_vector_type(4)));
typedef float v2f __attribute__((ext_vector_type(2)));
typedef float v4f __attribute__((ext_vector_type(4)));
typedef float v16f __attribute__((ext_vector_type(16)));
typedef __bf16 bf8 __attribute__((ext_vector_type(8)));
typedef __bf16 bf2 __attribute__((ext_vector_type(2)));
#define DI __device__ __forceinline__
#define LAS __attribute__((address_space(3)))
#define GAS __attribute__((address_space(1)))
#define SB() __builtin_amdgcn_sched_barrier(0)
#define PIN(x) asm volatile("" : "+v"(x))
#define DRAIN() asm volatile("s_waitcnt vmcnt(0)" ::: "memory")
#define VMWAIT(n) asm volatile("s_waitcnt vmcnt(" #n ")" ::: "memory")
#define RLX_AGENT __ATOMIC_RELAXED, __HIP_MEMORY_SCOPE_AGENT

constexpr int S = 2048, D = 512, F = 1792;
constexpr u32 MB = 1u << 20;
constexpr u32 FL = 128;
constexpr u32 C_E = 0, C_FW = 4096, C_FW2 = 6144, C_CNT = 8192, C_FB = 12288;
constexpr u32 C_FA = 65536, C_FG = C_FA + 256 * FL, C_FX = C_FG + 256 * FL, C_FH = C_FX + 256 * FL;
constexpr u32 OFF_MODP = 256 * 1024;
constexpr u32 OFF_KVT = 1 * MB;
constexpr u32 OFF_Z = 3 * MB;
constexpr u32 OFF_G = 5 * MB;
constexpr u32 OFF_X1 = 7 * MB;
constexpr u32 OFF_X1P = 9 * MB;
constexpr u32 OFF_H = 11 * MB;
constexpr u32 OFF_W1 = 18 * MB;
constexpr u32 OFF_W2 = OFF_W1 + 512 * 1024;
constexpr u32 OFF_W3 = 19 * MB;
constexpr u32 OFF_W4 = 21 * MB;
constexpr u32 WS_BYTES = 24 * MB;
constexpr u32 OOB = 0x80000000u;

constexpr int HC_STRIDE = 4160;
constexpr int L_HC = 0;
constexpr int L_U = 33280;
constexpr int U_STRIDE = 4608;
constexpr int L_T = 43008;
constexpr int L_TILE = 51200;
constexpr int L_CRED = 75776;
constexpr int L_Q = 108544;
constexpr int L_X = 0;
constexpr int L_ZS = 67584;
constexpr int L_STASH = 65536;
constexpr int L_RED4 = 0;
constexpr int L_XH = 131072;
constexpr int L_V = 147968;
constexpr int L_MISC = 158208;
constexpr int L_MODRED = L_MISC;
constexpr int L_CS3 = L_MISC;
constexpr int L_ST = L_MISC + 1024;
constexpr int L_ST1 = L_MISC + 3072;
constexpr int L_CNT = L_MISC + 3328;
constexpr int LDS_BYTES = L_CNT + 48;

#define STAMP(slot) do {} while (0)
#define STAMP_INIT() do {} while (0)
#define CSTAMP(slot) do {} while (0)

DI float lo2f(u32 x) { return __uint_as_float(x << 16); }
DI float hi2f(u32 x) { return __uint_as_float(x & 0xffff0000u); }
DI u32 pk2(float lo, float hi) { v2f t = {lo, hi}; bf2 b = __builtin_convertvector(t, bf2); return __builtin_bit_cast(u32, b); }
DI v4f mfma16(v4u a, v4u b, v4f c) { return __builtin_amdgcn_mfma_f32_16x16x32_bf16(__builtin_bit_cast(bf8, a), __builtin_bit_cast(bf8, b), c, 0, 0, 0); }
DI v16f mfma32(v4u a, v4u b, v16f c) { return __builtin_amdgcn_mfma_f32_32x32x16_bf16(__builtin_bit_cast(bf8, a), __builtin_bit_cast(bf8, b), c, 0, 0, 0); }
template <int CTRL> DI float dpp(float x) { return __builtin_bit_cast(float, __builtin_amdgcn_mov_dpp(__builtin_bit_cast(int, x), CTRL, 0xf, 0xf, true)); }
DI float red16(float x) { x += dpp<0xB1>(x); x += dpp<0x4E>(x); x += dpp<0x141>(x); x += dpp<0x140>(x); return x; }
DI float tr32(float a, float b) { auto r = __builtin_amdgcn_permlane32_swap(__float_as_uint(a), __float_as_uint(b), false, false); return __uint_as_float(r[0]) + __uint_as_float(r[1]); }
DI float tr16(float a, float b) { auto r = __builtin_amdgcn_permlane16_swap(__float_as_uint(a), __float_as_uint(b), false, false); return __uint_as_float(r[0]) + __uint_as_float(r[1]); }
DI float dot2(u32 a, u32 b, float c) { return __builtin_amdgcn_fdot2_f32_bf16(__builtin_bit_cast(bf2, a), __builtin_bit_cast(bf2, b), c, false); }
DI float rdl(float x, int l) { return __builtin_bit_cast(float, __builtin_amdgcn_readlane(__builtin_bit_cast(int, x), l)); }
struct Gelu { float k1, k2, irs; };
DI Gelu gelu_coef(float var) {
  const float v = fmaxf(var, 0.f) + 1e-5f, rs = __builtin_amdgcn_rsqf(v);
  return Gelu{-2.302208198f * rs, -0.1029432397f * rs * rs * rs, v * rs};
}
DI float gelu_s(float a, const Gelu& c) {
  const float e = __builtin_amdgcn_exp2f(a * __builtin_fmaf(a * a, c.k2, c.k1));
  float y = a * __builtin_amdgcn_rcpf(__builtin_fmaf(e, c.irs, c.irs));
  asm("" : "+v"(y));
  return y;
}
DI v2f pfma(v2f a, v2f b, v2f c) { return __builtin_elementwise_fma(a, b, c); }
DI v2f bc(float x) { return (v2f){x, x}; }
DI u32 pkv(v2f t) { bf2 b = __builtin_convertvector(t, bf2); return __builtin_bit_cast(u32, b); }
constexpr u32 ONES = 0x3f803f80u;
typedef __amdgpu_buffer_rsrc_t rsrc_t;
DI rsrc_t mk_rsrc(const void* p, u32 bytes) { return __builtin_amdgcn_make_buffer_rsrc((void*)p, 0, bytes, 0x00020000); }
DI v4u bload(rsrc_t r, u32 voff, u32 soff) { return __builtin_amdgcn_raw_buffer_load_b128(r, voff, soff, 0); }
DI v4u cload(rsrc_t r, u32 voff, u32 soff) { return __builtin_amdgcn_raw_buffer_load_b128(r, voff, soff, 16); }
DI v2u cload2(rsrc_t r, u32 voff, u32 soff) { return __builtin_amdgcn_raw_buffer_load_b64(r, voff, soff, 16); }
DI float cload1(rsrc_t r, u32 voff, u32 soff) { return __uint_as_float(__builtin_amdgcn_raw_buffer_load_b32(r, voff, soff, 16)); }
DI void cstore(v4u v, rsrc_t r, u32 voff) { __builtin_amdgcn_raw_buffer_store_b128(v, r, voff, 0, 16); }
DI void cstore1(float v, rsrc_t r, u32 voff) { __builtin_amdgcn_raw_buffer_store_b32(__float_as_uint(v), r, voff, 0, 16); }
DI u32 touch(rsrc_t r, u32 voff) { return __builtin_amdgcn_raw_buffer_load_b32(r, voff, 0, 16); }
#define KEEP(x) asm volatile("" :: "v"(x))
DI void bar() { __syncthreads(); }
template <int V> struct IC { static constexpr int value = V; };
template <int N, int I = 0, class Fn> DI void static_for(Fn&& f) { if constexpr (I < N) { f(IC<I>{}); static_for<N, I + 1>(f); } }

constexpr int SPIN_CAP = 1 << 17;
DI void poll(const char* ws, u32 off, bool active, u64 want) {
  GAS u64* p = (GAS u64*)(ws + off);
  for (int sp = 0; sp < SPIN_CAP; ++sp) {
    const u64 f = active ? __hip_atomic_load(p, RLX_AGENT) : want;
    if (__all(f == want)) break;
  }
}
DI void poll256(const char* ws, u32 off, int lane, u64 want) {
  GAS u64* p = (GAS u64*)(ws + off) + lane;
  for (int sp = 0; sp < SPIN_CAP; ++sp) {
    const u64 f0 = __hip_atomic_load(p, RLX_AGENT), f1 = __hip_atomic_load(p + 64, RLX_AGENT), f2 = __hip_atomic_load(p + 128, RLX_AGENT), f3 = __hip_atomic_load(p + 192, RLX_AGENT);
    if (__all(f0 == want && f1 == want && f2 == want && f3 == want)) break;
  }
}
DI bool all_tag(v4u f0, v4u f1, u64 tag) {
  const u32 tl = (u32)tag, th = (u32)(tag >> 32);
  return __all(f0[0] == tl && f0[1] == th && f0[2] == tl && f0[3] == th && f1[0] == tl && f1[1] == th && f1[2] == tl && f1[3] == th);
}
DI void post(char* ws, u32 off, u64 tag) { __hip_atomic_store((GAS u64*)(ws + off), tag, RLX_AGENT); }

struct Args { const u16 *q, *k, *v, *sh, *sk, *sD, *w1, *w2, *w3, *w4, *c, *wm; u16* out; char* ws; };

DI void ln_reduce(const float (&v)[8], float& mean, float& var) {
  const float a0 = tr32(v[0], v[4]), a1 = tr32(v[1], v[5]), a2 = tr32(v[2], v[6]), a3 = tr32(v[3], v[7]);
  mean = red16(tr16(a0, a2)) * (1.f / D);
  var = red16(tr16(a1, a3)) * (1.f / D) - mean * mean;
}
DI v2f ln_coef(float mean, float var) { const float r = __builtin_amdgcn_rsqf(fmaxf(var, 0.f) + 1e-5f); return (v2f){r, -mean * r}; }
DI v4u to_piece(u32 a0, u32 a1, u32 b0, u32 b1) {
  auto r0 = __builtin_amdgcn_permlane32_swap(a0, b0, false, false);
  auto r1 = __builtin_amdgcn_permlane32_swap(a1, b1, false, false);
  return (v4u){r0[0], r1[0], r0[1], r1[1]};
}

struct TileUnit { v4u tl[2]; u32 dst, str; bool wide; };
template <bool EARLY>
DI void tile_load(const Args& a, int u, bool valid, int lane, TileUnit& t) {
  const bool first = u < (EARLY ? 256 : 896);
  const u64 pa = (u64)(EARLY ? a.w1 : a.w3), pb = (u64)(EARLY ? a.w2 : a.w4);
  const u16* W = (const u16*)(pb + (first ? pa - pb : 0));
  const bool w3 = !EARLY && first;
  const int N = w3 ? F : D;
  const int ur = first ? u : u - (EARLY ? 256 : 896);
  const int s = w3 ? ur & 31 : ur >> 3, np = w3 ? ur >> 5 : ur & 7, nks2 = (EARLY || first) ? 16 : 56;
  t.str = (u32)nks2 * 1024;
  t.wide = w3;
  t.dst = valid ? (EARLY ? (first ? OFF_W1 : OFF_W2) : (first ? OFF_W3 : OFF_W4)) + (u32)(4 * np * nks2) * 1024 + (u32)s * (w3 ? 1024 : 512) : OOB;
  const rsrc_t rw = mk_rsrc(W, valid ? (u32)(D * (EARLY ? D : F) * 2) : 0u);
  const int jj = lane >> 3;
  const bool o1 = EARLY && first;
  const int rowl = o1 ? 128 * (((np >> 1) + (jj >> 1)) & 3) + (jj & 1) : 8 * (jj >> 2) + (jj & 3);
  const u32 vo = (u32)((rowl * N + 8 * (lane & 7)) * 2), so = (u32)(((o1 ? 4 : 16) * s * N + 64 * np) * 2), ho = (u32)((o1 ? 2 : 4) * N * 2);
  t.tl[0] = bload(rw, vo, so); t.tl[1] = bload(rw, vo, so + ho);
}
DI void tile_store(const TileUnit& t, char* tb, const rsrc_t rws, int lane) {
#pragma unroll
  for (int hh = 0; hh < 2; ++hh) *(v4u*)(tb + hh * 1024 + lane * 16) = t.tl[hh];
  const int hb = t.wide ? lane >> 5 : (lane >> 4) & 1, cb = t.wide ? (lane >> 4) & 1 : lane >> 5;
  const LAS char* ra = (const LAS char*)tb + (8 * hb + ((lane & 15) >> 2)) * 128 + cb * 32 + (lane & 3) * 8;
  const u32 lo_ = t.wide ? lane * 16 : (lane >> 5) * t.str + (lane & 31) * 16;
#pragma unroll
  for (int n2 = 0; n2 < 2; ++n2) {
    const v2u lo = __builtin_bit_cast(v2u, __builtin_amdgcn_ds_read_tr16_b64_v4i16((LAS v4s*)(ra + n2 * 64)));
    const v2u hi = __builtin_bit_cast(v2u, __builtin_amdgcn_ds_read_tr16_b64_v4i16((LAS v4s*)(ra + n2 * 64 + 512)));
    cstore((v4u){lo[0], lo[1], hi[0], hi[1]}, rws, t.dst + 2 * n2 * t.str + lo_);
  }
}
DI void conv_load_a(v4u (&A)[4][8], const char* lds, int w, int lane) {
  const int ch = w & 1, lh = w >> 2, m = lane & 15, kg = lane >> 4;
  const LAS char* hbase = (const LAS char*)lds + L_HC + ch * 4 * HC_STRIDE + (m & 3) * HC_STRIDE + 2 * (8 * kg + (m & 12)) + lh * 4 * 64;
#pragma unroll
  for (int lp = 0; lp < 4; ++lp)
#pragma unroll
    for (int j = 0; j < 8; ++j) {
      const v2u x0 = *(const volatile LAS v2u*)(hbase + lp * 64 + j * 512), x1 = *(const volatile LAS v2u*)(hbase + lp * 64 + j * 512 + 8);
      A[lp][j] = (v4u){x0[0], x0[1], x1[0], x1[1]};
    }
  SB();
}
DI bool arrive(char* lds, int slot, u32 n, int lane) {
  u32 arr = 0;
  if (lane == 0) arr = __hip_atomic_fetch_add((LAS u32*)(lds + L_CNT + slot * 4), 1u, __ATOMIC_ACQ_REL, __HIP_MEMORY_SCOPE_WORKGROUP);
  return __builtin_amdgcn_readfirstlane(arr) == n - 1;
}
DI void lds_wait(char* lds, int slot, u32 n) {
  for (int sp = 0; sp < SPIN_CAP && __hip_atomic_load((LAS u32*)(lds + L_CNT + slot * 4), __ATOMIC_ACQUIRE, __HIP_MEMORY_SCOPE_WORKGROUP) < n; ++sp) {}
}
DI void load_scalars(const Args& a, int d0, u64& tag, u32& dd) {
  const char* pe = a.ws + C_E; const u16* pd = a.sD + d0;
  asm volatile("" : "+s"(pe), "+s"(pd));
  tag = *(const __attribute__((address_space(4))) u64*)pe;
  dd = *(const __attribute__((address_space(4))) u32*)pd;
  asm volatile("" : "+s"(tag), "+s"(dd));
  tag += 1;
}
template <bool MOD>
DI void prep(const Args& a, char* lds, const rsrc_t rws, int b, int tid, int lane, int w, int d0, u64& tag, u32& dd, v4u (&A)[4][8]) {
  const int t2 = tid - 256, ch = t2 >> 7, t = t2 & 127, wq = w - 4;
  const rsrc_t rh = mk_rsrc(a.sk, S * D * 2);
  const u32 ho = (u32)((d0 + ch) * 4096 + t * 16);
  u32 cw = 0;
  {
    u64 pc; u32 len;
    asm volatile("s_getpc_b64 %0\n.Lmega_here%=:\n\ts_mov_b32 %1, .Lmega_code_end-.Lmega_here%=" : "=s"(pc), "=s"(len));
    cw = __builtin_amdgcn_raw_buffer_load_b32(mk_rsrc((const void*)pc, len), (w == 7 && lane < 8) ? (u32)((b >> 3) * 1024 + lane * 128) : OOB, 0, 0);
  }
  bar();
  v4u P[2], Q[2];
#pragma unroll
  for (int j = 0; j < 2; ++j) { P[j] = bload(rh, ho + j * 2048, 0); Q[j] = bload(rh, ho + j * 2048 + 16, 0); }
  SB();
#define PREP_AFTER_H 0
  TileUnit tu[2];
  v4u mx[4]; u32 cc[4];
  const int mrow = 128 * (b & 3) + 32 * wq + (lane >> 3);
  auto prep_loads = [&]() __attribute__((always_inline)) {
  if (MOD) {
    const rsrc_t rc = mk_rsrc(a.c, D * 2), rm = mk_rsrc(a.wm, D * 3072 * 2);
#pragma unroll
    for (int j = 0; j < 4; ++j) cc[j] = __builtin_amdgcn_raw_buffer_load_b16(rc, (u32)mrow * 2, j * 16, 0);
#pragma unroll
    for (int j = 0; j < 4; ++j) mx[j] = bload(rm, (u32)(mrow * 3072 + 64 * (b >> 2) + 8 * (lane & 7)) * 2, j * 8 * 3072 * 2);
  } else {
#pragma unroll
    for (int j = 0; j < 2; ++j) tile_load<true>(a, 8 * (b - 192) + 2 * wq + j, true, lane, tu[j]);
  }
  SB();
  };
  if (!PREP_AFTER_H) prep_loads();
  load_scalars(a, d0, tag, dd);
  if (t < 32) *(v4u*)(lds + L_U + ch * U_STRIDE + 4096 + t * 16) = (v4u){0, 0, 0, 0};
#pragma unroll
  for (int j = 0; j < 2; ++j) {
    const u32 a0 = __builtin_amdgcn_alignbit(P[j][1], P[j][0], 16), a1 = __builtin_amdgcn_alignbit(P[j][2], P[j][1], 16), a2 = __builtin_amdgcn_alignbit(P[j][3], P[j][2], 16),
              a3 = __builtin_amdgcn_alignbit(Q[j][0], P[j][3], 16), a4 = __builtin_amdgcn_alignbit(Q[j][1], Q[j][0], 16);
    char* hc = lds + L_HC + ch * 4 * HC_STRIDE + (t + 128 * j + 2) * 16;
    *(v4u*)(hc + 0 * HC_STRIDE) = (v4u){a0, a1, a2, a3};
    *(v4u*)(hc + 1 * HC_STRIDE) = (v4u){P[j][1], P[j][2], P[j][3], Q[j][0]};
    *(v4u*)(hc + 2 * HC_STRIDE) = (v4u){a1, a2, a3, a4};
    *(v4u*)(hc + 3 * HC_STRIDE) = (v4u){P[j][2], P[j][3], Q[j][0], Q[j][1]};
    if (j == 0 && t == 0) {
      char* h0 = lds + L_HC + ch * 4 * HC_STRIDE;
      const v4u z = {0, 0, 0, 0};
#pragma unroll
      for (int c = 0; c < 4; ++c) *(v4u*)(h0 + c * HC_STRIDE) = z;
      *(v4u*)(h0 + 16 + 0 * HC_STRIDE) = (v4u){0, 0, 0, P[0][0] << 16};
      *(v4u*)(h0 + 16 + 1 * HC_STRIDE) = (v4u){0, 0, 0, P[0][0]};
      *(v4u*)(h0 + 16 + 2 * HC_STRIDE) = (v4u){0, 0, P[0][0] << 16, a0};
      *(v4u*)(h0 + 16 + 3 * HC_STRIDE) = (v4u){0, 0, P[0][0], P[0][1]};
    }
  }
  KEEP(cw);
  arrive(lds, 5, 4, lane);
  STAMP(1);
  if (PREP_AFTER_H) { lds_wait(lds, 4, 4); prep_loads(); }
  if (MOD) {
    float acc[8] = {};
#pragma unroll
    for (int j = 0; j < 4; ++j) {
      const float c = lo2f(cc[j]); const float s = c * __builtin_amdgcn_rcpf(1.f + __builtin_amdgcn_exp2f(-1.442695041f * c));
      const u32 slo = pk2(s, 0.f), shi = slo << 16;
#pragma unroll
      for (int e = 0; e < 4; ++e) { acc[2 * e] = dot2(mx[j][e], slo, acc[2 * e]); acc[2 * e + 1] = dot2(mx[j][e], shi, acc[2 * e + 1]); }
    }
    const float b0 = tr32(acc[0], acc[4]), b1 = tr32(acc[1], acc[5]), b2 = tr32(acc[2], acc[6]), b3 = tr32(acc[3], acc[7]);
    float e0 = tr16(b0, b2), e1 = tr16(b1, b3);
    e0 += dpp<0x128>(e0); e1 += dpp<0x128>(e1);
    if (!(lane & 8)) *(v2f*)(lds + L_MODRED + wq * 256 + (8 * (lane & 7) + 2 * (lane >> 4)) * 4) = (v2f){e0, e1};
    if (arrive(lds, 0, 4, lane)) {
      const float* mr = (const float*)(lds + L_MODRED);
      cstore1(mr[lane] + mr[64 + lane] + mr[128 + lane] + mr[192 + lane], rws, OFF_MODP + (u32)((b & 3) * 3072 + 64 * (b >> 2) + lane) * 4);
      DRAIN();
      if (lane == 0) post(a.ws, C_FW + b * 8, tag);
    }
    STAMP(2);
    lds_wait(lds, 5, 4);
    conv_load_a(A, lds, w, lane);
  } else {
#pragma unroll
    for (int j = 0; j < 2; ++j) tile_store(tu[j], lds + L_TILE + (2 * wq + j) * 2048, rws, lane);
    lds_wait(lds, 5, 4);
    conv_load_a(A, lds, w, lane);
    DRAIN();
    if (arrive(lds, 0, 4, lane) && lane == 0) post(a.ws, C_FW + b * 8, tag);
    STAMP(2);
  }
}

__global__ __launch_bounds__(512, 2) void mega(const u16* pq, const u16* pk, const u16* pv, const u16* psh, const u16* psk, const u16* psD, const u16* pw1, const u16* pw2,
                                               const u16* pw3, const u16* pw4, const u16* pc, const u16* pwm, u16* pout, char* pws) {
  __shared__ __attribute__((aligned(16))) char lds[LDS_BYTES];
  asm volatile("" : "+s"(pq), "+s"(pk), "+s"(pv), "+s"(psh), "+s"(psk), "+s"(psD), "+s"(pw1), "+s"(pw2), "+s"(pw3), "+s"(pw4), "+s"(pc), "+s"(pwm), "+s"(pout), "+s"(pws));
  const Args a = {pq, pk, pv, psh, psk, psD, pw1, pw2, pw3, pw4, pc, pwm, pout, pws};
  const int b = blockIdx.x, tid = threadIdx.x, lane = tid & 63, w = __builtin_amdgcn_readfirstlane(tid >> 6);
  const int g = b & 7, bi = b >> 3;
  const rsrc_t rws = mk_rsrc(a.ws, WS_BYTES);
  STAMP_INIT(); STAMP(0);
  u64 tag;
  const int d0 = 64 * g + 2 * bi;
  const int tt = 8 * g + (bi >> 2), cgp = bi & 3;
  const int rg = 4 * g + (bi >> 3), cj = bi & 7;
  const int n = lane & 15, kg = lane >> 4;
  u32 dd;
  v4u A[4][8];
  TileUnit late;

  if (w < 4) {
    const int cg = tid & 7, rp = tid >> 3;
    const int r = 64 * bi + 2 * rp, c0 = 64 * g + 8 * cg;
    const rsrc_t rk = mk_rsrc(a.k, S * D * 2), rv = mk_rsrc(a.v, S * D * 2);
    v4u kk[5], vv[2], sc[4];
#pragma unroll
    for (int j = 0; j < 5; ++j) { const int rr = r - 3 + j; kk[j] = bload(rk, rr >= 0 ? (u32)((rr * D + c0) * 2) : OOB, 0); }
#pragma unroll
    for (int j = 0; j < 2; ++j) vv[j] = bload(rv, (u32)(((r + j) * D + c0) * 2), 0);
#pragma unroll
    for (int j = 0; j < 4; ++j) sc[j] = *(const GAS v4u*)(a.sh + c0 * 4 + 8 * j);
    SB();
    if (tid < 3) *(v4u*)(lds + L_CNT + tid * 16) = (v4u){0, 0, 0, 0};
    bar();
    load_scalars(a, d0, tag, dd);
    if (bi == 0 && tid == 0) post(a.ws, C_CNT + g * FL, 0);
    const int pos = 31 - rp;
    char* tb = lds + L_T + cg * 8 * 128 + ((((pos >> 2) ^ cg) << 2) + (pos & 3)) * 4;
#pragma unroll
    for (int c = 0; c < 8; ++c) {
      const u32 sel = (c & 1) ? 0x03020706u : 0x01000504u;
      u32 pr[4];
#pragma unroll
      for (int j = 0; j < 4; ++j) pr[j] = __builtin_amdgcn_perm(kk[j + 1][c >> 1], kk[j][c >> 1], sel);
      const u32 s01 = sc[c >> 1][(c & 1) * 2], s23 = sc[c >> 1][(c & 1) * 2 + 1];
      const float a0 = dot2(pr[2], s01, dot2(pr[0], s23, 0.f));
      const float a1 = dot2(pr[3], s01, dot2(pr[1], s23, 0.f));
      const float v0 = (c & 1) ? hi2f(vv[0][c >> 1]) : lo2f(vv[0][c >> 1]), v1 = (c & 1) ? hi2f(vv[1][c >> 1]) : lo2f(vv[1][c >> 1]);
      *(u32*)(tb + c * 128) = pk2(a1 * v1, a0 * v0);
    }
    arrive(lds, 6, 4, lane);
    lds_wait(lds, 6, 4);
    STAMP(1);
#pragma unroll
    for (int j = 0; j < 2; ++j) {
      const int ch = (tid >> 3) + 32 * j, ck = tid & 7;
      const v4u o = *(const v4u*)(lds + L_T + ch * 128 + ((ck ^ (ch >> 3)) * 16));
      cstore(o, rws, OFF_KVT + (u32)(64 * g + ch) * 4096 + (u32)(31 - bi) * 128 + ck * 16);
    }
    DRAIN();
    if (arrive(lds, 4, 4, lane) && lane == 0) post(a.ws, C_FA + (g * 32 + bi) * FL, tag);
    const u32 tk0 = touch(rws, lane < 16 ? OFF_KVT + (u32)(64 * g + 8 * w + (lane & 7) + 32 * (lane >> 3)) * 4096 + (u32)(31 - bi) * 128 : OOB);
    STAMP(2);
    lds_wait(lds, 5, 4);
    conv_load_a(A, lds, w, lane);
    KEEP(tk0);
    if (w == 0) {
      poll(a.ws, C_FA + (g * 32 + lane) * FL, lane < 32, tag);
      STAMP(56);
      v4u uu[8];
#pragma unroll
      for (int j = 0; j < 8; ++j) uu[j] = cload(rws, lane * 16, OFF_KVT + (u32)d0 * 4096 + j * 1024);
      SB();
#pragma unroll
      for (int j = 0; j < 8; ++j) *(v4u*)(lds + L_U + (j >> 2) * U_STRIDE + (j & 3) * 1024 + lane * 16) = uu[j];
    }
  } else if (b < 192) prep<true>(a, lds, rws, b, tid, lane, w, d0, tag, dd, A);
  else prep<false>(a, lds, rws, b, tid, lane, w, d0, tag, dd, A);
  STAMP(3);

  u32 qv[4][4];
  v2u mp[24];
  {
    const int ch = w & 1, pset = (w >> 1) & 1, lh = w >> 2;
    const LAS char* ubase = (const LAS char*)lds + L_U + ch * U_STRIDE + 2 * (2032 - 16 * n + 8 * kg) + lh * 4 * 64 - 7 * 512;
    bar();
    STAMP(4);
    v4f acc[4] = {};
    v4u B[8];
#pragma unroll
    for (int j = 0; j < 8; ++j) B[j] = *(const LAS v4u*)(ubase + (7 - j) * 512);
    v4u fw0, fw1, qs[4];
    const rsrc_t rq = mk_rsrc(a.q, S * D * 2);
    SB();
#define CONV_STEPS(P0, P1, P2, P3) \
    _Pragma("unroll") for (int lp = 0; lp < 4; ++lp) { \
      _Pragma("unroll") for (int qq = 0; qq <= P3; ++qq) { \
        if (qq <= P0) { acc[0] = mfma16(A[lp][P0 - qq], B[qq], acc[0]); PIN(acc[0]); } \
        if (qq <= P1) { acc[1] = mfma16(A[lp][P1 - qq], B[qq], acc[1]); PIN(acc[1]); } \
        if (qq <= P2) { acc[2] = mfma16(A[lp][P2 - qq], B[qq], acc[2]); PIN(acc[2]); } \
        acc[3] = mfma16(A[lp][P3 - qq], B[qq], acc[3]); PIN(acc[3]); \
        SB(); \
        if (lp < 3) { B[qq] = *(const LAS v4u*)(ubase + (lp + 1) * 64 + (7 - qq) * 512); SB(); } \
        if (lp == 0 && qq < 4) { qs[qq] = bload(rq, lane * 16, (u32)(32 * tt + 4 * w + qq) * 1024); SB(); } \
        if (lp == 2 && qq == 2) { tile_load<false>(a, 7 * b + w, w < 7, lane, late); SB(); } \
        if (lp == 2 && qq == 0) { fw0 = cload(rws, w < 4 ? lane * 16 : OOB, C_FW); fw1 = cload(rws, w < 4 ? lane * 16 : OOB, C_FW + 1024); SB(); } \
      } \
    }
    if (pset == 0) { CONV_STEPS(0, 3, 4, 7) } else { CONV_STEPS(1, 2, 5, 6) }
    STAMP(5);
    {
      char* red = lds + L_CRED + (lh * 2 + ch) * 8192;
      const int Ql = 4 * n + kg, Qs = Ql ^ ((Ql >> 3) & 3);
#pragma unroll
      for (int i = 0; i < 4; ++i) {
        const int p = pset == 0 ? (i == 0 ? 0 : i == 1 ? 3 : i == 2 ? 4 : 7) : (i == 0 ? 1 : i == 1 ? 2 : i == 2 ? 5 : 6);
        *(v4f*)(red + p * 1024 + Qs * 16) = acc[i];
      }
    }
#pragma unroll
    for (int t = 0; t < 4; ++t) *(v4u*)(lds + L_Q + (4 * w + t) * 1024 + lane * 16) = qs[t];
    if (w < 4) {
      if (!all_tag(fw0, fw1, tag)) poll256(a.ws, C_FW, lane, tag);
#pragma unroll
      for (int i = 0; i < 24; ++i) mp[i] = cload2(rws, tid * 8, OFF_MODP + (u32)((i & 3) * 3072 + (i >> 2) * D) * 4);
    }
    STAMP(45);
    bar();
    STAMP(46);
    {
      const char* red = lds + L_CRED + (tid ^ ((tid >> 3) & 3)) * 16;
      const v4f y0 = *(const v4f*)red + *(const v4f*)(red + 16384), y1 = *(const v4f*)(red + 8192) + *(const v4f*)(red + 24576);
      const v2u u0 = *(const v2u*)(lds + L_U + (2044 - 4 * tid) * 2), u1 = *(const v2u*)(lds + L_U + U_STRIDE + (2044 - 4 * tid) * 2);
      const u32 D0l = dd & 0xffffu, D0h = dd << 16, D1l = dd >> 16, D1h = dd & 0xffff0000u;
      const v4u o = {pk2(dot2(u0[1], D0h, y0[0]), dot2(u1[1], D1h, y1[0])), pk2(dot2(u0[1], D0l, y0[1]), dot2(u1[1], D1l, y1[1])),
                     pk2(dot2(u0[0], D0h, y0[2]), dot2(u1[0], D1h, y1[2])), pk2(dot2(u0[0], D0l, y0[3]), dot2(u1[0], D1l, y1[3]))};
      cstore(o, rws, OFF_Z + (u32)(tid >> 3) * 32768 + (u32)(d0 >> 1) * 128 + (tid & 7) * 16);
    }
    SB();
    if (w < 4) {
      v2f md[6];
#pragma unroll
      for (int i = 0; i < 6; ++i) { const u32 t = pkv((__builtin_bit_cast(v2f, mp[4 * i]) + __builtin_bit_cast(v2f, mp[4 * i + 1])) + (__builtin_bit_cast(v2f, mp[4 * i + 2]) + __builtin_bit_cast(v2f, mp[4 * i + 3]))); md[i] = (v2f){lo2f(t), hi2f(t)}; }
      v2f* V = (v2f*)(lds + L_V) + tid;
      V[0] = md[2] * (1.f + md[1]); V[D / 2] = md[2] * md[0]; V[D] = 1.f + md[4]; V[3 * D / 2] = md[3]; V[2 * D] = md[5];
    }
    STAMP(47);
    DRAIN();
    bar();
  }

  v4u wA[16], wB[16];
  const u32 w12o = (u32)(8 * cgp + w) * 16384;
  const int j8 = bi >> 2, j4 = bi >> 3;
#define LOAD_WA(lo, hi) do { SB(); _Pragma("unroll") for (int s_ = lo; s_ < hi; ++s_) wA[s_] = cload(rws, lane * 16, OFF_W1 + w12o + ((s_ + 2 * j8) & 15) * 1024); SB(); } while (0)
#define LOAD_WB(lo, hi) do { SB(); _Pragma("unroll") for (int s_ = lo; s_ < hi; ++s_) wB[s_] = cload(rws, lane * 16, OFF_W2 + w12o + ((s_ + 2 * j8) & 15) * 1024); SB(); } while (0)
  u32 tch[2], wrm[4];
#define WARM(r) do { SB(); wrm[r] = touch(rws, (w < 7 && lane < 32) ? ((r) < 2 ? OFF_W3 : OFF_W4) + (u32)((((r) & 1) * 32 + bi) * 224 + w * 32 + lane) * 128 : OOB); SB(); } while (0)
  v2f x[4][4];
  u32 gm[4][4][2];
  v2f Cv[4], Ev[4];
  {
    u64 old = 0;
    if (tid == 448) old = __hip_atomic_fetch_add((GAS u64*)(a.ws + C_CNT + g * FL), 1ull, RLX_AGENT);
    if (w == 7 && __builtin_amdgcn_readfirstlane((u32)old) == 31 && lane < 8) post(a.ws, C_FB + (lane * 8 + g) * FL, tag);
    SB();
    STAMP(6);
    const u32 wvo = w < 7 ? lane * 16 : OOB;
#define LOAD_WA7(lo, hi) do { SB(); _Pragma("unroll") for (int s_ = lo; s_ < hi; ++s_) wA[s_] = cload(rws, wvo, OFF_W1 + w12o + ((s_ + 2 * j8) & 15) * 1024); SB(); } while (0)
    tile_store(late, lds + L_TILE + w * 2048, rws, lane);
    LOAD_WA7(0, 4);
#pragma unroll
    for (int ci = 0; ci < 4; ++ci)
#pragma unroll
      for (int t = 0; t < 4; ++t) qv[ci][t] = *(const u32*)(lds + L_Q + (4 * w + t) * 1024 + ((cgp + ci) & 3) * 256 + lane * 4);
    float st[8] = {}, mean, var;
#pragma unroll
    for (int ci = 0; ci < 4; ++ci)
#pragma unroll
      for (int t = 0; t < 4; ++t) { st[2 * t] = dot2(qv[ci][t], ONES, st[2 * t]); st[2 * t + 1] = dot2(qv[ci][t], qv[ci][t], st[2 * t + 1]); }
#pragma unroll
    for (int i = 0; i < 8; ++i) PIN(st[i]);
    LOAD_WA7(4, 8);
    ln_reduce(st, mean, var);
    const v2f l1 = ln_coef(mean, var);
#pragma unroll
    for (int ci = 0; ci < 4; ++ci) {
      LOAD_WA7(8 + 2 * ci, 10 + 2 * ci);
      const float* Vr = (const float*)(lds + L_V) + 128 * ((cgp + ci) & 3) + 2 * lane;
      const v2f Av = *(const v2f*)Vr, Bv = *(const v2f*)(Vr + D);
      Cv[ci] = *(const v2f*)(Vr + 2 * D); Ev[ci] = *(const v2f*)(Vr + 3 * D);
#pragma unroll
      for (int t = 0; t < 4; ++t) {
        x[ci][t] = (v2f){lo2f(qv[ci][t]), hi2f(qv[ci][t])};
        const v2f gg = pfma(pfma(x[ci][t], bc(rdl(l1[0], 16 * t)), bc(rdl(l1[1], 16 * t))), Av, Bv);
        gm[ci][t][0] = pk2(gg[0], 0.f); gm[ci][t][1] = pk2(0.f, gg[1]); PIN(gm[ci][t][0]); PIN(gm[ci][t][1]); PIN(x[ci][t]);
      }
    }
    SB();
  }
#pragma unroll
  for (int s = 0; s < 4; ++s) wB[s] = cload(rws, w < 7 ? lane * 16 : OOB, OFF_W2 + w12o + ((s + 2 * j8) & 15) * 1024);
  SB();
  VMWAIT(20);
  if (w == 7) poll(a.ws, C_FB + (g * 8 + lane) * FL, lane < 8, tag);
  STAMP(7);
  bar();
  STAMP(8);
  v4u w3[32];
  const u32 w3o = OFF_W3 + (u32)(7 * cj + w) * 32768, w3v = w < 7 ? lane * 16 : OOB;
#define LOAD_W3(lo, hi) do { SB(); _Pragma("unroll") for (int s_ = lo; s_ < hi; ++s_) w3[s_] = cload(rws, w3v, w3o + ((s_ + 8 * j4) & 31) * 1024); SB(); } while (0)
  const v4u ones = {ONES, ONES, ONES, ONES};
  v4f cs1 = {};
  {
    v4u zs[4];
#pragma unroll
    for (int i = 0; i < 4; ++i) zs[i] = cload(rws, tid * 16, OFF_Z + (u32)tt * 32768 + i * 8192);
    LOAD_WB(4, 7);
    if (tid == 0) { post(a.ws, C_FW2 + b * 8, tag); if (b == 0) post(a.ws, C_E, tag); }
    SB();
#pragma unroll
    for (int i = 0; i < 4; ++i) *(v4u*)(lds + L_ZS + i * 8192 + (tid ^ ((tid >> 4) & 7)) * 16) = zs[i];
    SB();
    if (w == 7) { LOAD_WA(0, 16); LOAD_WB(0, 4); }
    WARM(0);
    bar();
    STAMP(9);
    v4u z[4];
#pragma unroll
    for (int ci = 0; ci < 4; ++ci) z[ci] = *(const v4u*)(lds + L_ZS + ((cgp + ci) & 3) * 8192 + lane * 128 + ((w ^ ((lane >> 1) & 7)) * 16));
    v2f s1[4], s2[4];
#pragma unroll
    for (int ci = 0; ci < 4; ++ci) {
#pragma unroll
      for (int t = 0; t < 4; ++t) x[ci][t] = (v2f){dot2(z[ci][t], gm[ci][t][0], x[ci][t][0]), dot2(z[ci][t], gm[ci][t][1], x[ci][t][1])};
#pragma unroll
      for (int t = 0; t < 4; ++t) {
        if (ci == 0) { s1[t] = x[ci][t]; s2[t] = x[ci][t] * x[ci][t]; } else { s1[t] += x[ci][t]; s2[t] = pfma(x[ci][t], x[ci][t], s2[t]); }
        PIN(s1[t]); PIN(s2[t]);
      }
    }
    STAMP(42);
    float st[8], mean, var;
#pragma unroll
    for (int t = 0; t < 4; ++t) { st[2 * t] = s1[t][0] + s1[t][1]; st[2 * t + 1] = s2[t][0] + s2[t][1]; PIN(st[2 * t]); PIN(st[2 * t + 1]); }
    ln_reduce(st, mean, var);
    const v2f l2 = ln_coef(mean, var);
#pragma unroll
    for (int t = 0; t < 4; ++t) *(u32*)(lds + L_XH + (4 * w + t) * 264 + lane * 4) = pkv(x[0][t]);
    const v4u f20 = cload(rws, w == 0 ? lane * 16 : OOB, C_FW2), f21 = cload(rws, w == 0 ? lane * 16 : OOB, C_FW2 + 1024);
    STAMP(43);
#pragma unroll
    for (int t = 0; t < 4; ++t) {
      const float rs = rdl(l2[0], 16 * t), mo = rdl(l2[1], 16 * t);
      v4u o;
#pragma unroll
      for (int ci = 0; ci < 4; ++ci) { x[ci][t] = pfma(pfma(x[ci][t], bc(rs), bc(mo)), Cv[ci], Ev[ci]); o[ci] = pkv(x[ci][t]); }
      *(v4u*)(lds + L_X + (((lane >> 1) - 4 * j8) & 31) * 1024 + ((((lane & 1) * 32 + 4 * w + t) ^ (lane & 7)) * 16)) = o;
      *(u32*)(lds + L_XH + 32 * 264 + (4 * w + t) * 264 + lane * 4) = o[0];
    }
    SB();
#pragma unroll
    for (int s = 0; s < 16; ++s) cs1 = mfma16(wA[s], ones, cs1);
    PIN(cs1);
    if (w == 0 && !all_tag(f20, f21, tag)) poll256(a.ws, C_FW2, lane, tag);
    WARM(1);
    STAMP(44);
    bar();
    STAMP(10);
  }
  const u32 xfo = (u32)((kg >> 1) * 1024 + (kg & 1) * 512 + n * 16);
  const u32 pco = (u32)(((lane >> 4) & 1) * 512 + ((lane & 15) + 16 * (lane >> 5)) * 16);
  const u32 fro = (u32)tt * 32768 + (u32)(8 * cgp + w) * 1024 + pco;
  {
    v4f acc[2] = {};
    v4u xf[8];
    const char* xb0 = lds + L_X + (kg >> 1) * 1024 + (((kg & 1) * 32 + n) ^ kg) * 16;
    const char* xb1 = lds + L_X + (kg >> 1) * 1024 + (((kg & 1) * 32 + n) ^ (4 + kg)) * 16;
#pragma unroll
    for (int i = 0; i < 8; ++i) xf[i] = *(const v4u*)((((i >> 1) & 1) ? xb1 : xb0) + (i >> 1) * 2048 + (i & 1) * 256);
    float st[8] = {};
    SB();
#pragma unroll
    for (int i = 0; i < 16; ++i) {
      acc[i & 1] = mfma16(wA[i >> 1], xf[i & 7], acc[i & 1]); PIN(acc[i & 1]);
      xf[i & 7] = *(const v4u*)((((i >> 1) & 1) ? xb1 : xb0) + ((i + 8) >> 1) * 2048 + (i & 1) * 256);
#pragma unroll
      for (int e = 0; e < 2; ++e) { const float y = x[i >> 2][i & 3][e]; st[2 * (i & 3)] += y; st[2 * (i & 3) + 1] = __builtin_fmaf(y, y, st[2 * (i & 3) + 1]); }
      PIN(st[2 * (i & 3)]); PIN(st[2 * (i & 3) + 1]);
      SB();
    }
    STAMP(32);
    float mean, var;
    ln_reduce(st, mean, var);
    if (n == 0) *(v2f*)(lds + L_ST1 + (4 * w + kg) * 8) = (v2f){mean, var};
#pragma unroll
    for (int i = 16; i < 32; ++i) {
      acc[i & 1] = mfma16(wA[i >> 1], xf[i & 7], acc[i & 1]); PIN(acc[i & 1]);
      if (i < 24) xf[i & 7] = *(const v4u*)((((i >> 1) & 1) ? xb1 : xb0) + ((i + 8) >> 1) * 2048 + (i & 1) * 256);
    }
    LOAD_WB(7, 10);
    STAMP(33);
    bar();
    STAMP(34);
    u32 pk[2][2];
#pragma unroll
    for (int tb = 0; tb < 2; ++tb) {
      const v2f s3 = *(const v2f*)(lds + L_ST1 + (16 * tb + n) * 8);
      const Gelu gc = gelu_coef(s3[1]);
      const v4f y = acc[tb] - s3[0] * cs1;
      pk[tb][0] = pk2(gelu_s(y[0], gc), gelu_s(y[1], gc));
      pk[tb][1] = pk2(gelu_s(y[2], gc), gelu_s(y[3], gc));
    }
    cstore(to_piece(pk[0][0], pk[0][1], pk[1][0], pk[1][1]), rws, OFF_G + fro);
    STAMP(35);
    DRAIN();
    KEEP(wrm[0]); KEEP(wrm[1]);
    tch[0] = touch(rws, OFF_G + fro);
    STAMP(36);
    bar();
    if (tid == 448) post(a.ws, C_FG + (tt * 4 + cgp) * FL, tag);
    STAMP(11);
  }
  if (w < 7) { LOAD_WB(10, 16); LOAD_W3(0, 5); }
  else {
    const int ft = 4 * (tt - 1) + lane;
    poll(a.ws, C_FG + ft * FL, lane < 12 && ft >= 0 && ft < 256, tag);
  }
  bar();
  STAMP(12);
  {
    const int sg = w >> 1;
    const int t = 32 * tt + (lane & 31) + (sg == 0 ? -1 : sg == 1 ? 1 : sg == 2 ? -2 : 2);
    const u32 off = (t >= 0 && t < S) ? (u32)((t >> 5) * 32768 + ((lane >> 5) * 32 + (t & 31)) * 16) : OOB;
    v4u xs[4];
#pragma unroll
    for (int i = 0; i < 4; ++i) xs[i] = cload(rws, off, OFF_G + (u32)(4 * w + i) * 1024);
    SB();
    v4f acc[2];
#pragma unroll
    for (int tb = 0; tb < 2; ++tb) {
      const v2u hv = *(const v2u*)(lds + L_XH + 32 * 264 + (16 * tb + n) * 264 + (16 * w + 4 * kg) * 2);
      acc[tb] = (v4f){lo2f(hv[0]), hi2f(hv[0]), lo2f(hv[1]), hi2f(hv[1])}; PIN(acc[tb]);
    }
    const char* xh = lds + L_XH + ((lane & 15) + 16 * (lane >> 5)) * 264 + (16 * w + 4 * ((lane >> 4) & 1)) * 2;
    const v2u xa = *(const v2u*)xh, xc = *(const v2u*)(xh + 16);
    SB();
#pragma unroll
    for (int i = 0; i < 4; ++i) *(v4u*)(lds + L_X + (4 * ((w - j8) & 7) + i) * 1024 + lane * 16) = xs[i];
    KEEP(tch[0]);
    if (w == 7) LOAD_WB(10, 16);
    LOAD_W3(5, 9);
    bar();
    STAMP(13);
    v4u xf[8];
    const char* xb = lds + L_X + xfo;
#pragma unroll
    for (int i = 0; i < 8; ++i) xf[i] = *(const v4u*)(xb + (i >> 1) * 2048 + (i & 1) * 256);
    cstore((v4u){xa[0], xa[1], xc[0], xc[1]}, rws, OFF_X1 + fro);
    SB();
#pragma unroll
    for (int i = 0; i < 32; ++i) {
      acc[i & 1] = mfma16(wB[i >> 1], xf[i & 7], acc[i & 1]); PIN(acc[i & 1]);
      if (i < 24) xf[i & 7] = *(const v4u*)(xb + ((i + 8) >> 1) * 2048 + (i & 1) * 256);
      if (i % 3 == 0 && i < 27) LOAD_W3(9 + i / 3, 10 + i / 3);
      SB();
    }
    cstore(to_piece(pk2(acc[0][0], acc[0][1]), pk2(acc[0][2], acc[0][3]), pk2(acc[1][0], acc[1][1]), pk2(acc[1][2], acc[1][3])), rws, OFF_X1P + fro);
    STAMP(40);
    DRAIN();
    tch[0] = touch(rws, OFF_X1P + fro); tch[1] = touch(rws, OFF_X1 + fro);
    STAMP(41);
    bar();
    if (tid == 448) post(a.ws, C_FX + (tt * 4 + cgp) * FL, tag);
    STAMP(14);
  }
  v4u w4[28];
  u32 ks[7];
#pragma unroll
  for (int s = 0; s < 7; ++s) { const int t = s + (cj == 7 ? 0 : cj); ks[s] = (u32)(t >= 7 ? t - 7 : t) * 1024; }
  const u32 w4o = OFF_W4 + (u32)(4 * cj * 56 + 7 * w) * 1024;
#define LOAD_W4(idx) do { w4[idx] = cload(rws, lane * 16, w4o + (u32)(((idx) / 7 + j4) & 3) * (56 * 1024) + ks[(idx) % 7]); SB(); } while (0)
  const u32 fo4 = (u32)(w >> 2) * 32768 + (u32)(4 * cj + (w & 3)) * 1024 + (u32)((kg & 1) * 512 + n * 16 + (kg >> 1) * 8);
  v2u x1f[2], xpf[2];
  if (w < 7) {
    LOAD_W3(18, 32);
    const u32 sv = ((kg & 1) == (n >> 3)) ? ONES : 0u;
    const v4u sel = {sv, sv, sv, sv};
    v4f c0 = {}, c1 = {};
#pragma unroll
    for (int s = 0; s < 32; s += 2) { c0 = mfma16(w3[s], sel, c0); c1 = mfma16(w3[s + 1], sel, c1); }
    if ((n & 7) == 0) *(v4f*)(lds + L_CS3 + w * 128 + (n >> 3) * 64 + kg * 16) = c0 + c1;
  } else poll(a.ws, C_FX + (rg * 8 + lane) * FL, lane < 8, tag);
  bar();
  STAMP(15);
  {
    v4u xs[8];
#pragma unroll
    for (int i = 0; i < 8; ++i) xs[i] = cload(rws, lane * 16, OFF_X1P + (u32)rg * 65536 + (u32)(8 * w + i) * 1024);
    SB();
    float s1 = 0, s2 = 0;
#pragma unroll
    for (int i = 0; i < 8; ++i) {
      *(v4u*)(lds + L_X + ((w & 4) * 8 + 8 * ((w - j4) & 3) + i) * 1024 + lane * 16) = xs[i];
#pragma unroll
      for (int e = 0; e < 4; ++e) { s1 = dot2(xs[i][e], ONES, s1); s2 = dot2(xs[i][e], xs[i][e], s2); }
      PIN(s1); PIN(s2);
      SB();
    }
    *(float*)(lds + L_ST + w * 256 + (lane & 31) * 8 + (lane >> 5) * 4) = tr32(s1, s2);
    KEEP(tch[0]); KEEP(tch[1]);
    LOAD_W4(0); LOAD_W4(1);
    STAMP(49);
    bar();
    STAMP(16);
    v4u xf[14];
    const u32 xo = OFF_H + (u32)(2 * rg) * (112 * 1024) + (u32)(14 * w) * 1024;
#define LOAD_XF(p, lo, hi) do { SB(); _Pragma("unroll") for (int s_ = lo; s_ < hi; ++s_) xf[s_] = cload(rws, xfo + (s_ & 1) * 256, xo + (p) * 112 * 1024 + 2 * ks[s_ >> 1]); SB(); } while (0)
#define READ_XPF() do { _Pragma("unroll") for (int tb = 0; tb < 2; ++tb) { xpf[tb] = *(const v2u*)(lds + L_X + (w >> 2) * 32768 + ((4 * cj + (w & 3) - 8 * j4) & 31) * 1024 + (fo4 & 1023) + tb * 256); PIN(xpf[tb]); } } while (0)
    if (w == 7) {
      READ_XPF();
#pragma unroll
      for (int s = 2; s < 28; ++s) { LOAD_W4(s); __builtin_amdgcn_s_sleep(1); }
      tch[0] = 0; tch[1] = 0;
    } else {
      const u32 ho = OFF_H + (u32)(2 * rg) * (112 * 1024) + (u32)(2 * (7 * cj + w)) * 1024;
      const char* xb = lds + L_X + lane * 16;
      v4u xa[6];
#pragma unroll
      for (int i = 0; i < 6; ++i) xa[i] = *(const v4u*)(xb + (i & 1) * 32768 + (i >> 1) * 1024);
      SB();
      v4f cs[4];
      Gelu gc[2]; float mn[2];
      v16f acc[2] = {};
      static_for<64>([&](auto I) __attribute__((always_inline)) {
        constexpr int fi = I.value, p = fi & 1, i = fi >> 1;
        acc[p] = mfma32(w3[i], xa[fi % 6], acc[p]); PIN(acc[p]);
        if constexpr (fi + 6 < 64) xa[fi % 6] = *(const v4u*)(xb + p * 32768 + ((fi + 6) >> 1) * 1024);
        if constexpr (fi == 40 || fi == 44) {
          constexpr int t = fi == 40 ? 0 : 1;
          const char* sp = lds + L_ST + t * 1024 + (lane & 31) * 8;
          v2f st = (*(const v2f*)sp + *(const v2f*)(sp + 256)) + (*(const v2f*)(sp + 512) + *(const v2f*)(sp + 768));
          st *= (1.f / D);
          gc[t] = gelu_coef(st[1] - st[0] * st[0]); mn[t] = -st[0];
          PIN(gc[t].k1); PIN(gc[t].k2); PIN(gc[t].irs); PIN(mn[t]);
        }
        if constexpr (fi == 58) {
#pragma unroll
          for (int q = 0; q < 4; ++q) cs[q] = *(const v4f*)(lds + L_CS3 + w * 128 + (8 * q + 4 * (lane >> 5)) * 4);
          READ_XPF();
        }
        SB();
        if constexpr (p == 1 && i < 26) LOAD_W4(2 + i);
      });
      STAMP(17);
#pragma unroll
      for (int p = 0; p < 2; ++p)
#pragma unroll
        for (int s = 0; s < 2; ++s) {
          v4u o;
#pragma unroll
          for (int e = 0; e < 4; ++e) {
            const int r = 8 * s + 2 * e;
            o[e] = pk2(gelu_s(__builtin_fmaf(cs[r >> 2][r & 3], mn[p], acc[p][r]), gc[p]), gelu_s(__builtin_fmaf(cs[r >> 2][(r & 3) + 1], mn[p], acc[p][r + 1]), gc[p]));
          }
          cstore(o, rws, ho + p * 112 * 1024 + s * 1024 + lane * 16);
          SB();
        }
      STAMP(27);
      DRAIN();
      tch[0] = touch(rws, ho + lane * 32); tch[1] = touch(rws, ho + 112 * 1024 + lane * 32);
    }
#pragma unroll
    for (int tb = 0; tb < 2; ++tb) x1f[tb] = cload2(rws, fo4 + tb * 256, OFF_X1 + (u32)rg * 65536);
    STAMP(18);
    bar();
    if (w == 7) {
      if (lane == 0) post(a.ws, C_FH + (rg * 8 + cj) * FL, tag);
      poll(a.ws, C_FH + (rg * 8 + lane) * FL, lane < 8, tag);
    }
    bar();
    STAMP(19);
    LOAD_XF(0, 0, 14);
    KEEP(tch[0]); KEEP(tch[1]);
#pragma unroll
    for (int p = 0; p < 2; ++p) {
      v4f acc[4][2] = {};
#pragma unroll
      for (int s = 0; s < 14; ++s) {
#pragma unroll
        for (int mt = 0; mt < 4; ++mt) { acc[mt][s & 1] = mfma16(w4[7 * mt + (s >> 1)], xf[s], acc[mt][s & 1]); PIN(acc[mt][s & 1]); }
        if (p == 0) LOAD_XF(1, s, s + 1);
        SB();
      }
      if (p == 1) { STAMP(54); } else { STAMP(55); }
#pragma unroll
      for (int mt = 0; mt < 4; ++mt)
#pragma unroll
        for (int tb = 0; tb < 2; ++tb) *(v4f*)(lds + L_RED4 + ((4 * p + ((mt + j4) & 3)) ^ 4) * 16384 + w * 2048 + tb * 1024 + lane * 16) = acc[mt][tb];
      SB();
      if (p == 0) { STAMP(22); }
    }
    bar();
    STAMP(20);
    const int f0 = 64 * cj + 16 * (w & 3);
    const v4f gb = *(const v4f*)((const float*)(lds + L_V) + 4 * D + f0 + 4 * kg);
    u32 o[2][2];
#pragma unroll
    for (int tb = 0; tb < 2; ++tb) {
      v4f m = {lo2f(xpf[tb][0]), hi2f(xpf[tb][0]), lo2f(xpf[tb][1]), hi2f(xpf[tb][1])};
#pragma unroll
      for (int sw = 0; sw < 8; ++sw) m += *(const v4f*)(lds + L_RED4 + (w ^ 4) * 16384 + sw * 2048 + tb * 1024 + lane * 16);
      o[tb][0] = pk2(lo2f(x1f[tb][0]) + gb[0] * m[0], hi2f(x1f[tb][0]) + gb[1] * m[1]);
      o[tb][1] = pk2(lo2f(x1f[tb][1]) + gb[2] * m[2], hi2f(x1f[tb][1]) + gb[3] * m[3]);
    }
    auto e0 = __builtin_amdgcn_permlane16_swap(o[0][0], o[1][0], false, false);
    auto e1 = __builtin_amdgcn_permlane16_swap(o[0][1], o[1][1], false, false);
    *(GAS v4u*)(a.out + (size_t)(64 * rg + 32 * (w >> 2) + n + 16 * (kg & 1)) * D + f0 + 8 * (kg >> 1)) = (v4u){e0[0], e1[0], e0[1], e1[1]};
  }
  asm volatile(".Lmega_code_end:");
}

extern "C" void kernel_launch(void* const* d_in, const int* in_sizes, int n_in, void* d_out, int out_size, void* d_ws, size_t ws_size, hipStream_t stream) {
  mega<<<256, 512, 0, stream>>>((const u16*)d_in[0], (const u16*)d_in[1], (const u16*)d_in[2], (const u16*)d_in[3], (const u16*)d_in[4], (const u16*)d_in[5],
                                (const u16*)d_in[7], (const u16*)d_in[8], (const u16*)d_in[9], (const u16*)d_in[10], (const u16*)d_in[11], (const u16*)d_in[12],
                                (u16*)d_out, (char*)d_ws);
}
```
